```python
import torch

def _softmax(x, axis=-1):
    return torch.softmax(x, dim=axis)
def _gelu(x):
    return torch.nn.functional.gelu(x, approximate="tanh")
def _silu(x):
    return torch.nn.functional.silu(x)
def _sigmoid(x):
    return torch.sigmoid(x)
def _layernorm(x, axis=-1, eps=1e-5):
    m = x.mean(dim=axis, keepdim=True)
    v = x.var(dim=axis, keepdim=True, unbiased=False)
    return (x - m) / torch.sqrt(v + eps)
def _scatter_add(tgt, idx, src, axis=0):
    return tgt.clone().index_add_(axis, idx, src)
def _segment_sum(src, seg, n):
    out = torch.zeros((n,) + src.shape[1:], dtype=src.dtype)
    return out.index_add_(0, seg, src)
def _pad(x, pw):
    flat = tuple(v for pair in reversed(pw) for v in pair)
    return torch.nn.functional.pad(x, flat)

kernel_name = 'recurrent_scan__mha_block_cached__moe_arch_mod__prompt'

def setup_inputs():

    _w = (lambda a: (lambda t: t.to(torch.bfloat16) if t.is_floating_point() else t)(torch.as_tensor(a)))
    def _A_setup():
        B, Sq, D = 1, 2048, 512
        H, Sk, Dh = 32, 2048, 128
        return {
            "x": _w((lambda *s: torch.randn(*s, dtype=torch.bfloat16) * 0.05)(B, Sq, D)),
            "k_cache": _w((lambda *s: torch.randn(*s, dtype=torch.bfloat16) * 1.6)(B, H, Sk, Dh)),
            "v_cache": _w((lambda *s: torch.randn(*s, dtype=torch.bfloat16) * 0.2)(B, H, Sk, Dh)),
            "wq": _w((lambda *s: torch.randn(*s, dtype=torch.bfloat16) * 1.6)(D, H * Dh) * (D ** -0.5)),
            "wo": _w((lambda *s: torch.randn(*s, dtype=torch.bfloat16) * 0.2)(H * Dh, D) * ((H * Dh) ** -0.5)),
        }
    _A_in = _A_setup()

    def _B_setup():
        B, S, D, F, C = 1, 2048, 512, 1376, 1
        return {
            "x": _w((lambda *s: torch.randn(*s, dtype=torch.bfloat16) * 0.05)(B, S, D)),
            "w_r": _w((lambda *s: torch.randn(*s, dtype=torch.bfloat16) * 0.2)(D)),
            "w_gate": _w((lambda *s: torch.randn(*s, dtype=torch.bfloat16) * 0.1)(D, F)),
            "w_up": _w((lambda *s: torch.randn(*s, dtype=torch.bfloat16) * 0.1)(D, F)),
            "w_down": _w((lambda *s: torch.randn(*s, dtype=torch.bfloat16) * 0.1)(F, D)),
        }
    _B_in = _B_setup()

    return {
        "A_x": _A_in["x"],
        "A_k_cache": _A_in["k_cache"],
        "A_v_cache": _A_in["v_cache"],
        "A_wq": _A_in["wq"],
        "A_wo": _A_in["wo"],
        "B_x": _B_in["x"],
        "B_w_r": _B_in["w_r"],
        "B_w_gate": _B_in["w_gate"],
        "B_w_up": _B_in["w_up"],
        "B_w_down": _B_in["w_down"],
    }

def reference(A_x, A_k_cache, A_v_cache, A_wq, A_wo, B_x, B_w_r, B_w_gate, B_w_up, B_w_down):
    def _A_ref(x, k_cache, v_cache, wq, wo):
        B, Sq, D = x.shape
        H, Dh = 32, 128
        q = (lambda x, *d: x.permute(*d))((x @ wq).reshape(B, Sq, H, Dh), 0, 2, 1, 3)
        scores = torch.einsum("bhqd,bhkd->bhqk", q, k_cache) * (Dh ** -0.5)
        p = _softmax(scores, axis=-1)
        o = torch.einsum("bhqk,bhkd->bhqd", p, v_cache)
        return (lambda x, *d: x.permute(*d))(o, 0, 2, 1, 3).reshape(B, Sq, H * Dh) @ wo

    def _B_ref(x, w_r, w_gate, w_up, w_down):
        B, S, D = x.shape
        C = 1
        r = x @ w_r
        idx = torch.argsort(-r, axis=-1)[:, :C]
        idx_e = (lambda x, n, axis: torch.repeat_interleave(x, n, dim=axis))(idx.reshape(B, C, 1), D, axis=2)
        x_sel = (lambda x, i, axis: torch.take_along_dim(x, i, dim=axis))(x, idx_e, axis=1)
        g = _silu(x_sel @ w_gate)
        u = x_sel @ w_up
        y = (g * u) @ w_down
        r_sel = _sigmoid((lambda x, i, axis: torch.take_along_dim(x, i, dim=axis))(r, idx, axis=1))
        y = y * r_sel.reshape(B, C, 1)
        b_off = (torch.arange(B) * S).reshape(B, 1)
        flat_idx = (idx + b_off).reshape(B * C)
        zero = (x * 0.0).reshape(B * S, D)
        upd = _scatter_add(zero, flat_idx, y.reshape(B * C, D), axis=0)
        return x + upd.reshape(B, S, D)

    x = A_x
    xn = _layernorm(x, axis=-1)
    s = _A_ref(xn, A_k_cache, A_v_cache, A_wq, A_wo)
    g = _silu(_B_ref(xn, B_w_r, B_w_gate, B_w_up, B_w_down))
    return x + s * g
```

```cpp
#include <hip/hip_runtime.h>
#include <hip/hip_bf16.h>

typedef float f32x16 __attribute__((ext_vector_type(16)));
typedef float f32x4 __attribute__((ext_vector_type(4)));
typedef float f32x2 __attribute__((ext_vector_type(2)));
typedef __bf16 bf16x8 __attribute__((ext_vector_type(8)));
typedef __bf16 bf16x2 __attribute__((ext_vector_type(2)));
typedef unsigned u32x4 __attribute__((ext_vector_type(4)));
#define DI __device__ __forceinline__

constexpr unsigned FS = 136192u, WS_WF = 50331648u, WS_M = 20971520u, WS_R = WS_M + 16384u, WS_Y = WS_M + 1048576u, WS_G = WS_M + 2097152u, WS_F = WS_M + 24576u, WS_P = 33554432u;
constexpr int NHELP = 172;

__global__ __launch_bounds__(256, 1) void k_attn(const void* x, const void* kc, const void* vc, const void* wq, const void* wo, const void* wr, void* ws, const void* wg, const void* wu) {
  __shared__ __attribute__((aligned(16))) char lds[163840];
  asm volatile(
    "v_lshrrev_b32 v2, 6, %8\n"
    "v_and_b32 v1, 63, %8\n"
    "s_nop 0\n"
    "v_readfirstlane_b32 s64, v2\n"
    "s_and_b32 s73, %7, 7\n"
    "s_lshl_b32 s73, s73, 2\n"
    "s_bfe_u32 s74, %7, 0x20003\n"
    "s_or_b32 s65, s73, s74\n"
    "s_lshr_b32 s66, %7, 5\n"
    "s_mov_b32 s79, 0x3f803f80\n"
    "s_mov_b32 s72, 0\n"
    "s_getpc_b64 s[52:53]\n"
    "LPC%=:\n"
    "s_mov_b32 s54, LEND%=-LPC%=+19968\n"
    "s_mov_b32 s55, 0x20000\n"
    "s_lshr_b32 s73, %7, 3\n"
    "s_lshl_b32 s73, s73, 2\n"
    "s_add_u32 s73, s73, s64\n"
    "s_lshl_b32 s73, s73, 10\n"
    "v_lshlrev_b32 v23, 4, v1\n"
    "buffer_load_dwordx4 v[252:255], v23, s[52:55], s73 offen\n"
    "s_mov_b64 s[32:33], %0\n"
    "s_mov_b32 s34, 2097152\n"
    "s_mov_b32 s35, 0x20000\n"
    "s_lshl_b32 s73, s66, 8\n"
    "s_lshl_b32 s74, s65, 3\n"
    "s_add_u32 s73, s73, s74\n"
    "s_lshl_b32 s74, s64, 1\n"
    "s_add_u32 s81, s73, s74\n"
    "s_lshl_b32 s73, s81, 10\n"
    "v_lshlrev_b32 v23, 4, v1\n"
    "buffer_load_dwordx4 v[192:195], v23, s[32:35], s73 offen\n"
    "buffer_load_dwordx4 v[196:199], v23, s[32:35], s73 offen offset:1024\n"
    "global_load_dwordx4 v[200:203], v23, %5\n"
    "s_lshl_b32 s73, s65, 8\n"
    "s_sub_u32 s75, 4198400, s73\n"
    "s_sub_u32 s73, s73, 4096\n"
    "s_subb_u32 s74, 0, 0\n"
    "s_mov_b64 s[36:37], %3\n"
    "s_add_u32 s36, s36, s73\n"
    "s_addc_u32 s37, s37, s74\n"
    "s_mov_b32 s38, s75\n"
    "s_mov_b32 s39, 0x20000\n"
    "v_lshrrev_b32 v25, 5, v1\n"
    "v_bfe_u32 v26, v1, 2, 3\n"
    "v_and_b32 v27, 3, v1\n"
    "v_and_b32 v28, 15, v1\n"
    "v_lshrrev_b32 v29, 4, v1\n"
    "v_lshlrev_b32 v30, 6, v25\n"
    "v_lshl_add_u32 v31, v27, 4, v30\n"
    "v_lshl_add_u32 v19, v26, 13, v31\n"
    "v_lshl_add_u32 v20, v26, 10, v31\n"
    "v_xor_b32 v21, 48, v20\n"
    "s_lshl_b32 s77, s66, 18\n"
    "s_lshl_b32 s73, s64, 16\n"
    "s_add_u32 s77, s77, s73\n"
    "s_lshl_b32 s70, s64, 12\n"
    "s_add_u32 s71, s70, 65536\n"
    "s_lshl_b32 s76, s64, 17\n"
    "s_and_b32 s73, s64, 1\n"
    "s_lshl_b32 s73, s73, 13\n"
    "s_cmp_ge_u32 s64, 2\n"
    "s_cselect_b32 s82, 6, 5\n"
    "s_cselect_b32 s83, 9, 7\n"
    "s_cselect_b32 s78, 4, 1\n"
    "s_lshl_b32 s82, s82, 14\n"
    "s_add_u32 s82, s82, s73\n"
    "s_lshl_b32 s83, s83, 14\n"
    "s_add_u32 s83, s83, s73\n"
    "s_lshl_b32 s78, s78, 14\n"
    "s_add_u32 s78, s78, s73\n"
    "s_add_u32 m0, s70, 32768\n"
    "s_add_u32 s73, s76, 4096\n"
    "buffer_load_dwordx4 v19, s[36:39], s73 offen offset:0 lds\n"
    "s_add_u32 s73, s76, 3200\n"
    "buffer_load_dwordx4 v19, s[36:39], s73 offen offset:1024 lds\n"
    "s_add_u32 s73, s76, 67584\n"
    "buffer_load_dwordx4 v19, s[36:39], s73 offen offset:2048 lds\n"
    "s_add_u32 s73, s76, 66688\n"
    "buffer_load_dwordx4 v19, s[36:39], s73 offen offset:3072 lds\n"
    "s_add_u32 m0, s82, 0\n"
    "s_add_u32 s73, s77, 0\n"
    "buffer_load_dwordx4 v20, s[32:35], s73 offen offset:0 lds\n"
    "s_add_u32 s73, s77, 7168\n"
    "buffer_load_dwordx4 v21, s[32:35], s73 offen offset:1024 lds\n"
    "s_add_u32 s73, s77, 14336\n"
    "buffer_load_dwordx4 v20, s[32:35], s73 offen offset:2048 lds\n"
    "s_add_u32 s73, s77, 21504\n"
    "buffer_load_dwordx4 v21, s[32:35], s73 offen offset:3072 lds\n"
    "s_add_u32 m0, s82, 4096\n"
    "s_add_u32 s73, s77, 32768\n"
    "buffer_load_dwordx4 v20, s[32:35], s73 offen offset:0 lds\n"
    "s_add_u32 s73, s77, 39936\n"
    "buffer_load_dwordx4 v21, s[32:35], s73 offen offset:1024 lds\n"
    "s_add_u32 s73, s77, 47104\n"
    "buffer_load_dwordx4 v20, s[32:35], s73 offen offset:2048 lds\n"
    "s_add_u32 s73, s77, 54272\n"
    "buffer_load_dwordx4 v21, s[32:35], s73 offen offset:3072 lds\n"
    "s_lshl_b32 s73, s65, 19\n"
    "s_sub_u32 s73, s73, 1024\n"
    "s_subb_u32 s74, 0, 0\n"
    "s_mov_b64 s[40:41], %1\n"
    "s_add_u32 s40, s40, s73\n"
    "s_addc_u32 s41, s41, s74\n"
    "s_mov_b32 s42, 525312\n"
    "s_mov_b32 s43, 0x20000\n"
    "s_mov_b64 s[44:45], %2\n"
    "s_add_u32 s44, s44, s73\n"
    "s_addc_u32 s45, s45, s74\n"
    "s_mov_b32 s46, 525312\n"
    "s_mov_b32 s47, 0x20000\n"
    "s_mov_b64 s[48:49], %4\n"
    "s_mov_b32 s50, 4194304\n"
    "s_mov_b32 s51, 0x20000\n"
    "s_mov_b64 s[52:53], %6\n"
    "s_mov_b32 s54, 17432576\n"
    "s_mov_b32 s55, 0x20000\n"
    "s_mov_b64 s[56:57], %6\n"
    "s_add_u32 s56, s56, 50331648\n"
    "s_addc_u32 s57, s57, 0\n"
    "s_mov_b32 s58, 4358144\n"
    "s_mov_b32 s59, 0x20000\n"
    "s_mov_b64 s[60:61], %6\n"
    "s_add_u32 s60, s60, 20971520\n"
    "s_addc_u32 s61, s61, 0\n"
    "s_mov_b32 s62, 8388608\n"
    "s_mov_b32 s63, 0x20000\n"
    "v_lshl_add_u32 v31, v26, 8, v31\n"
    "v_add_u32 v5, 1024, v31\n"
    "v_add_u32 v6, 128, v31\n"
    "v_xor_b32 v7, 48, v5\n"
    "v_xor_b32 v8, 48, v6\n"
    "v_lshrrev_b32 v30, 2, v26\n"
    "v_lshlrev_b32 v30, 5, v30\n"
    "v_xor_b32 v9, v30, v5\n"
    "v_xor_b32 v10, v30, v6\n"
    "v_lshrrev_b32 v30, 3, v28\n"
    "v_mul_u32_u24 v31, 3, v30\n"
    "v_xor_b32 v31, v31, v29\n"
    "v_lshlrev_b32 v31, 4, v31\n"
    "v_and_b32 v25, 7, v28\n"
    "v_lshl_add_u32 v31, v25, 6, v31\n"
    "v_lshl_add_u32 v2, v30, 11, v31\n"
    "v_lshl_add_u32 v24, v30, 10, v31\n"
    "v_add_u32 v24, s82, v24\n"
    "v_bfe_u32 v30, v1, 2, 2\n"
    "v_lshlrev_b32 v30, 6, v30\n"
    "v_lshl_add_u32 v31, v27, 4, v30\n"
    "v_lshl_add_u32 v31, v29, 11, v31\n"
    "v_add_u32 v22, 32768, v31\n"
    "s_lshl_b32 s73, s64, 9\n"
    "v_add_u32 v23, s73, v22\n"
    "v_lshl_add_u32 v30, v27, 3, v30\n"
    "v_and_b32 v31, 1, v29\n"
    "v_mul_u32_u24 v31, 288, v31\n"
    "v_add_u32 v30, v30, v31\n"
    "v_lshrrev_b32 v31, 1, v29\n"
    "v_lshl_add_u32 v30, v31, 11, v30\n"
    "v_add_u32 v3, 65536, v30\n"
    "v_xor_b32 v4, 32, v3\n"
    "s_mul_i32 s80, s65, 0\n"
    "s_add_u32 s80, s80, s70\n"
    "s_add_u32 m0, s70, 49152\n"
    "s_add_u32 s73, s76, 528384\n"
    "buffer_load_dwordx4 v19, s[36:39], s73 offen offset:0 lds\n"
    "s_add_u32 s73, s76, 527488\n"
    "buffer_load_dwordx4 v19, s[36:39], s73 offen offset:1024 lds\n"
    "s_add_u32 s73, s76, 591872\n"
    "buffer_load_dwordx4 v19, s[36:39], s73 offen offset:2048 lds\n"
    "s_add_u32 s73, s76, 590976\n"
    "buffer_load_dwordx4 v19, s[36:39], s73 offen offset:3072 lds\n"
    "s_add_u32 m0, s83, 0\n"
    "s_add_u32 s73, s77, 128\n"
    "buffer_load_dwordx4 v20, s[32:35], s73 offen offset:0 lds\n"
    "s_add_u32 s73, s77, 7296\n"
    "buffer_load_dwordx4 v21, s[32:35], s73 offen offset:1024 lds\n"
    "s_add_u32 s73, s77, 14464\n"
    "buffer_load_dwordx4 v20, s[32:35], s73 offen offset:2048 lds\n"
    "s_add_u32 s73, s77, 21632\n"
    "buffer_load_dwordx4 v21, s[32:35], s73 offen offset:3072 lds\n"
    "s_add_u32 m0, s83, 4096\n"
    "s_add_u32 s73, s77, 32896\n"
    "buffer_load_dwordx4 v20, s[32:35], s73 offen offset:0 lds\n"
    "s_add_u32 s73, s77, 40064\n"
    "buffer_load_dwordx4 v21, s[32:35], s73 offen offset:1024 lds\n"
    "s_add_u32 s73, s77, 47232\n"
    "buffer_load_dwordx4 v20, s[32:35], s73 offen offset:2048 lds\n"
    "s_add_u32 s73, s77, 54400\n"
    "buffer_load_dwordx4 v21, s[32:35], s73 offen offset:3072 lds\n"
    "s_waitcnt vmcnt(24)\n"
    "v_mov_b32 v204, 0\n"
    "v_mov_b32 v205, 0\n"
    "v_mov_b32 v206, 0\n"
    "v_mov_b32 v207, 0\n"
    "v_mov_b32 v208, 0\n"
    "v_mov_b32 v209, 0\n"
    "v_mov_b32 v210, 0\n"
    "v_dot2_f32_bf16 v204, v192, s79, v204\n"
    "v_dot2_f32_bf16 v205, v192, v192, v205\n"
    "v_dot2_f32_bf16 v206, v192, v200, v206\n"
    "v_dot2_f32_bf16 v207, v196, s79, v207\n"
    "v_dot2_f32_bf16 v208, v196, v196, v208\n"
    "v_dot2_f32_bf16 v209, v196, v200, v209\n"
    "v_dot2_f32_bf16 v210, v200, s79, v210\n"
    "v_dot2_f32_bf16 v204, v193, s79, v204\n"
    "v_dot2_f32_bf16 v205, v193, v193, v205\n"
    "v_dot2_f32_bf16 v206, v193, v201, v206\n"
    "v_dot2_f32_bf16 v207, v197, s79, v207\n"
    "v_dot2_f32_bf16 v208, v197, v197, v208\n"
    "v_dot2_f32_bf16 v209, v197, v201, v209\n"
    "v_dot2_f32_bf16 v210, v201, s79, v210\n"
    "v_dot2_f32_bf16 v204, v194, s79, v204\n"
    "v_dot2_f32_bf16 v205, v194, v194, v205\n"
    "v_dot2_f32_bf16 v206, v194, v202, v206\n"
    "v_dot2_f32_bf16 v207, v198, s79, v207\n"
    "v_dot2_f32_bf16 v208, v198, v198, v208\n"
    "v_dot2_f32_bf16 v209, v198, v202, v209\n"
    "v_dot2_f32_bf16 v210, v202, s79, v210\n"
    "v_dot2_f32_bf16 v204, v195, s79, v204\n"
    "v_dot2_f32_bf16 v205, v195, v195, v205\n"
    "v_dot2_f32_bf16 v206, v195, v203, v206\n"
    "v_dot2_f32_bf16 v207, v199, s79, v207\n"
    "v_dot2_f32_bf16 v208, v199, v199, v208\n"
    "v_dot2_f32_bf16 v209, v199, v203, v209\n"
    "v_dot2_f32_bf16 v210, v203, s79, v210\n"
    "s_nop 2\n"
    "v_mov_b32 v29, s79\n"
    "v_accvgpr_write_b32 a232, v29\n"
    "v_accvgpr_write_b32 a233, v29\n"
    "v_accvgpr_write_b32 a234, v29\n"
    "v_accvgpr_write_b32 a235, v29\n"
    "s_waitcnt vmcnt(12)\n"
    "s_barrier\n"
    "ds_read_b128 v[32:35], v24 offset:0\n"
    "ds_read_b128 v[36:39], v24 offset:2048\n"
    "ds_read_b128 v[40:43], v24 offset:4096\n"
    "ds_read_b128 v[44:47], v24 offset:6144\n"
    "ds_read_b64_tr_b16 a[128:129], v22 offset:0\n"
    "ds_read_b64_tr_b16 a[130:131], v22 offset:256\n"
    "ds_read_b64_tr_b16 a[132:133], v22 offset:8\n"
    "ds_read_b64_tr_b16 a[134:135], v22 offset:264\n"
    "ds_read_b64_tr_b16 a[136:137], v22 offset:512\n"
    "ds_read_b64_tr_b16 a[138:139], v22 offset:768\n"
    "ds_read_b64_tr_b16 a[140:141], v22 offset:520\n"
    "ds_read_b64_tr_b16 a[142:143], v22 offset:776\n"
    "ds_read_b64_tr_b16 a[144:145], v22 offset:1024\n"
    "ds_read_b64_tr_b16 a[146:147], v22 offset:1280\n"
    "ds_read_b64_tr_b16 a[148:149], v22 offset:1032\n"
    "ds_read_b64_tr_b16 a[150:151], v22 offset:1288\n"
    "ds_read_b64_tr_b16 a[152:153], v22 offset:1536\n"
    "ds_read_b64_tr_b16 a[154:155], v22 offset:1792\n"
    "ds_read_b64_tr_b16 a[156:157], v22 offset:1544\n"
    "ds_read_b64_tr_b16 a[158:159], v22 offset:1800\n"
    "ds_read_b64_tr_b16 a[192:193], v23 offset:0\n"
    "ds_read_b64_tr_b16 a[194:195], v23 offset:256\n"
    "ds_read_b64_tr_b16 a[196:197], v23 offset:8\n"
    "ds_read_b64_tr_b16 a[198:199], v23 offset:264\n"
    "s_waitcnt lgkmcnt(0)\n"
    "v_mfma_f32_16x16x32_bf16 v[64:67], a[128:131], v[32:35], 0\n"
    "ds_read_b128 v[48:51], v24 offset:512\n"
    "v_mfma_f32_16x16x32_bf16 v[96:99], a[128:131], v[36:39], 0\n"
    "ds_read_b128 v[52:55], v24 offset:2560\n"
    "v_mfma_f32_16x16x32_bf16 v[128:131], a[128:131], v[40:43], 0\n"
    "ds_read_b128 v[56:59], v24 offset:4608\n"
    "v_mfma_f32_16x16x32_bf16 v[160:163], a[128:131], v[44:47], 0\n"
    "ds_read_b128 v[60:63], v24 offset:6656\n"
    "v_mfma_f32_16x16x32_bf16 v[68:71], a[132:135], v[32:35], 0\n"
    "ds_read_b64_tr_b16 a[160:161], v22 offset:8192\n"
    "v_mfma_f32_16x16x32_bf16 v[100:103], a[132:135], v[36:39], 0\n"
    "ds_read_b64_tr_b16 a[162:163], v22 offset:8448\n"
    "v_mfma_f32_16x16x32_bf16 v[132:135], a[132:135], v[40:43], 0\n"
    "ds_read_b64_tr_b16 a[164:165], v22 offset:8200\n"
    "v_mfma_f32_16x16x32_bf16 v[164:167], a[132:135], v[44:47], 0\n"
    "ds_read_b64_tr_b16 a[166:167], v22 offset:8456\n"
    "v_mfma_f32_16x16x32_bf16 v[72:75], a[136:139], v[32:35], 0\n"
    "ds_read_b64_tr_b16 a[168:169], v22 offset:8704\n"
    "v_mfma_f32_16x16x32_bf16 v[104:107], a[136:139], v[36:39], 0\n"
    "ds_read_b64_tr_b16 a[170:171], v22 offset:8960\n"
    "v_mfma_f32_16x16x32_bf16 v[136:139], a[136:139], v[40:43], 0\n"
    "ds_read_b64_tr_b16 a[172:173], v22 offset:8712\n"
    "v_mfma_f32_16x16x32_bf16 v[168:171], a[136:139], v[44:47], 0\n"
    "ds_read_b64_tr_b16 a[174:175], v22 offset:8968\n"
    "v_mfma_f32_16x16x32_bf16 v[76:79], a[140:143], v[32:35], 0\n"
    "ds_read_b64_tr_b16 a[176:177], v22 offset:9216\n"
    "v_mfma_f32_16x16x32_bf16 v[108:111], a[140:143], v[36:39], 0\n"
    "ds_read_b64_tr_b16 a[178:179], v22 offset:9472\n"
    "v_mfma_f32_16x16x32_bf16 v[140:143], a[140:143], v[40:43], 0\n"
    "ds_read_b64_tr_b16 a[180:181], v22 offset:9224\n"
    "v_mfma_f32_16x16x32_bf16 v[172:175], a[140:143], v[44:47], 0\n"
    "ds_read_b64_tr_b16 a[182:183], v22 offset:9480\n"
    "v_mfma_f32_16x16x32_bf16 v[80:83], a[144:147], v[32:35], 0\n"
    "ds_read_b64_tr_b16 a[184:185], v22 offset:9728\n"
    "v_mfma_f32_16x16x32_bf16 v[112:115], a[144:147], v[36:39], 0\n"
    "ds_read_b64_tr_b16 a[186:187], v22 offset:9984\n"
    "v_mfma_f32_16x16x32_bf16 v[144:147], a[144:147], v[40:43], 0\n"
    "ds_read_b64_tr_b16 a[188:189], v22 offset:9736\n"
    "v_mfma_f32_16x16x32_bf16 v[176:179], a[144:147], v[44:47], 0\n"
    "ds_read_b64_tr_b16 a[190:191], v22 offset:9992\n"
    "v_mfma_f32_16x16x32_bf16 v[84:87], a[148:151], v[32:35], 0\n"
    "ds_read_b64_tr_b16 a[200:201], v23 offset:8192\n"
    "v_mfma_f32_16x16x32_bf16 v[116:119], a[148:151], v[36:39], 0\n"
    "ds_read_b64_tr_b16 a[202:203], v23 offset:8448\n"
    "v_mfma_f32_16x16x32_bf16 v[148:151], a[148:151], v[40:43], 0\n"
    "ds_read_b64_tr_b16 a[204:205], v23 offset:8200\n"
    "v_mfma_f32_16x16x32_bf16 v[180:183], a[148:151], v[44:47], 0\n"
    "ds_read_b64_tr_b16 a[206:207], v23 offset:8456\n"
    "v_mfma_f32_16x16x32_bf16 v[88:91], a[152:155], v[32:35], 0\n"
    "s_add_u32 m0, s70, 131072\n"
    "s_add_u32 s73, s76, 1052672\n"
    "buffer_load_dwordx4 v19, s[36:39], s73 offen offset:0 lds\n"
    "v_mfma_f32_16x16x32_bf16 v[120:123], a[152:155], v[36:39], 0\n"
    "s_add_u32 s73, s76, 1051776\n"
    "buffer_load_dwordx4 v19, s[36:39], s73 offen offset:1024 lds\n"
    "v_mfma_f32_16x16x32_bf16 v[152:155], a[152:155], v[40:43], 0\n"
    "s_add_u32 s73, s76, 1116160\n"
    "buffer_load_dwordx4 v19, s[36:39], s73 offen offset:2048 lds\n"
    "v_mfma_f32_16x16x32_bf16 v[184:187], a[152:155], v[44:47], 0\n"
    "s_add_u32 s73, s76, 1115264\n"
    "buffer_load_dwordx4 v19, s[36:39], s73 offen offset:3072 lds\n"
    "v_mfma_f32_16x16x32_bf16 v[92:95], a[156:159], v[32:35], 0\n"
    "s_add_u32 m0, s78, 0\n"
    "s_add_u32 s73, s77, 256\n"
    "buffer_load_dwordx4 v20, s[32:35], s73 offen offset:0 lds\n"
    "v_mfma_f32_16x16x32_bf16 v[124:127], a[156:159], v[36:39], 0\n"
    "s_add_u32 s73, s77, 7424\n"
    "buffer_load_dwordx4 v21, s[32:35], s73 offen offset:1024 lds\n"
    "v_mfma_f32_16x16x32_bf16 v[156:159], a[156:159], v[40:43], 0\n"
    "s_add_u32 s73, s77, 14592\n"
    "buffer_load_dwordx4 v20, s[32:35], s73 offen offset:2048 lds\n"
    "v_mfma_f32_16x16x32_bf16 v[188:191], a[156:159], v[44:47], 0\n"
    "s_add_u32 s73, s77, 21760\n"
    "buffer_load_dwordx4 v21, s[32:35], s73 offen offset:3072 lds\n"
    "v_mfma_f32_16x16x32_bf16 a[208:211], a[232:235], a[192:195], 0\n"
    "s_add_u32 m0, s78, 4096\n"
    "s_add_u32 s73, s77, 33024\n"
    "buffer_load_dwordx4 v20, s[32:35], s73 offen offset:0 lds\n"
    "v_mfma_f32_16x16x32_bf16 a[212:215], a[232:235], a[196:199], 0\n"
    "s_add_u32 s73, s77, 40192\n"
    "buffer_load_dwordx4 v21, s[32:35], s73 offen offset:1024 lds\n"
    "v_mfma_f32_16x16x32_bf16 a[216:219], a[232:235], v[32:35], 0\n"
    "s_add_u32 s73, s77, 47360\n"
    "buffer_load_dwordx4 v20, s[32:35], s73 offen offset:2048 lds\n"
    "v_mfma_f32_16x16x32_bf16 a[220:223], a[232:235], v[36:39], 0\n"
    "s_add_u32 s73, s77, 54528\n"
    "buffer_load_dwordx4 v21, s[32:35], s73 offen offset:3072 lds\n"
    "v_mfma_f32_16x16x32_bf16 a[224:227], a[232:235], v[40:43], 0\n"
    "s_add_u32 m0, s70, 0\n"
    "s_add_u32 s74, s80, 0\n"
    "s_and_b32 s74, s74, 0x7ffff\n"
    "buffer_load_dwordx4 v5, s[40:43], s74 offen offset:0 lds\n"
    "v_mfma_f32_16x16x32_bf16 a[228:231], a[232:235], v[44:47], 0\n"
    "buffer_load_dwordx4 v6, s[40:43], s74 offen offset:1024 lds\n"
    "v_mfma_f32_16x16x32_bf16 a[236:239], v[32:35], v[32:35], 0\n"
    "buffer_load_dwordx4 v7, s[40:43], s74 offen offset:2048 lds\n"
    "v_mfma_f32_16x16x32_bf16 a[240:243], v[36:39], v[36:39], 0\n"
    "buffer_load_dwordx4 v8, s[40:43], s74 offen offset:3072 lds\n"
    "v_mfma_f32_16x16x32_bf16 a[244:247], v[40:43], v[40:43], 0\n"
    "v_mfma_f32_16x16x32_bf16 a[248:251], v[44:47], v[44:47], 0\n"
    "s_waitcnt lgkmcnt(0)\n"
    "s_waitcnt vmcnt(16)\n"
    "s_barrier\n"
    "v_add_u32 v22, 16384, v22\n"
    "v_add_u32 v23, 16384, v23\n"
    "s_sub_u32 s75, s83, s82\n"
    "v_add_u32 v24, s75, v24\n"
    "v_mfma_f32_16x16x32_bf16 v[64:67], a[160:163], v[48:51], v[64:67]\n"
    "ds_read_b128 v[32:35], v24 offset:0\n"
    "v_add_f32_dpp v204, v204, v204 row_ror:8 row_mask:0xf bank_mask:0xf\n"
    "v_mfma_f32_16x16x32_bf16 v[96:99], a[160:163], v[52:55], v[96:99]\n"
    "ds_read_b128 v[36:39], v24 offset:2048\n"
    "v_add_f32_dpp v205, v205, v205 row_ror:8 row_mask:0xf bank_mask:0xf\n"
    "v_mfma_f32_16x16x32_bf16 v[128:131], a[160:163], v[56:59], v[128:131]\n"
    "ds_read_b128 v[40:43], v24 offset:4096\n"
    "v_add_f32_dpp v206, v206, v206 row_ror:8 row_mask:0xf bank_mask:0xf\n"
    "v_mfma_f32_16x16x32_bf16 v[160:163], a[160:163], v[60:63], v[160:163]\n"
    "ds_read_b128 v[44:47], v24 offset:6144\n"
    "v_add_f32_dpp v207, v207, v207 row_ror:8 row_mask:0xf bank_mask:0xf\n"
    "v_mfma_f32_16x16x32_bf16 v[68:71], a[164:167], v[48:51], v[68:71]\n"
    "ds_read_b64_tr_b16 a[128:129], v22 offset:0\n"
    "v_add_f32_dpp v208, v208, v208 row_ror:8 row_mask:0xf bank_mask:0xf\n"
    "v_mfma_f32_16x16x32_bf16 v[100:103], a[164:167], v[52:55], v[100:103]\n"
    "ds_read_b64_tr_b16 a[130:131], v22 offset:256\n"
    "v_add_f32_dpp v209, v209, v209 row_ror:8 row_mask:0xf bank_mask:0xf\n"
    "v_mfma_f32_16x16x32_bf16 v[132:135], a[164:167], v[56:59], v[132:135]\n"
    "ds_read_b64_tr_b16 a[132:133], v22 offset:8\n"
    "v_add_f32_dpp v210, v210, v210 row_ror:8 row_mask:0xf bank_mask:0xf\n"
    "v_mfma_f32_16x16x32_bf16 v[164:167], a[164:167], v[60:63], v[164:167]\n"
    "ds_read_b64_tr_b16 a[134:135], v22 offset:264\n"
    "v_add_f32_dpp v204, v204, v204 row_ror:4 row_mask:0xf bank_mask:0xf\n"
    "v_mfma_f32_16x16x32_bf16 v[72:75], a[168:171], v[48:51], v[72:75]\n"
    "ds_read_b64_tr_b16 a[136:137], v22 offset:512\n"
    "v_add_f32_dpp v205, v205, v205 row_ror:4 row_mask:0xf bank_mask:0xf\n"
    "v_mfma_f32_16x16x32_bf16 v[104:107], a[168:171], v[52:55], v[104:107]\n"
    "ds_read_b64_tr_b16 a[138:139], v22 offset:768\n"
    "v_add_f32_dpp v206, v206, v206 row_ror:4 row_mask:0xf bank_mask:0xf\n"
    "v_mfma_f32_16x16x32_bf16 v[136:139], a[168:171], v[56:59], v[136:139]\n"
    "ds_read_b64_tr_b16 a[140:141], v22 offset:520\n"
    "v_add_f32_dpp v207, v207, v207 row_ror:4 row_mask:0xf bank_mask:0xf\n"
    "v_mfma_f32_16x16x32_bf16 v[168:171], a[168:171], v[60:63], v[168:171]\n"
    "ds_read_b64_tr_b16 a[142:143], v22 offset:776\n"
    "v_add_f32_dpp v208, v208, v208 row_ror:4 row_mask:0xf bank_mask:0xf\n"
    "v_mfma_f32_16x16x32_bf16 v[76:79], a[172:175], v[48:51], v[76:79]\n"
    "ds_read_b64_tr_b16 a[144:145], v22 offset:1024\n"
    "v_add_f32_dpp v209, v209, v209 row_ror:4 row_mask:0xf bank_mask:0xf\n"
    "v_mfma_f32_16x16x32_bf16 v[108:111], a[172:175], v[52:55], v[108:111]\n"
    "ds_read_b64_tr_b16 a[146:147], v22 offset:1280\n"
    "v_add_f32_dpp v210, v210, v210 row_ror:4 row_mask:0xf bank_mask:0xf\n"
    "v_mfma_f32_16x16x32_bf16 v[140:143], a[172:175], v[56:59], v[140:143]\n"
    "ds_read_b64_tr_b16 a[148:149], v22 offset:1032\n"
    "v_add_f32_dpp v204, v204, v204 row_ror:2 row_mask:0xf bank_mask:0xf\n"
    "v_mfma_f32_16x16x32_bf16 v[172:175], a[172:175], v[60:63], v[172:175]\n"
    "ds_read_b64_tr_b16 a[150:151], v22 offset:1288\n"
    "v_add_f32_dpp v205, v205, v205 row_ror:2 row_mask:0xf bank_mask:0xf\n"
    "v_mfma_f32_16x16x32_bf16 v[80:83], a[176:179], v[48:51], v[80:83]\n"
    "ds_read_b64_tr_b16 a[152:153], v22 offset:1536\n"
    "v_add_f32_dpp v206, v206, v206 row_ror:2 row_mask:0xf bank_mask:0xf\n"
    "v_mfma_f32_16x16x32_bf16 v[112:115], a[176:179], v[52:55], v[112:115]\n"
    "ds_read_b64_tr_b16 a[154:155], v22 offset:1792\n"
    "v_add_f32_dpp v207, v207, v207 row_ror:2 row_mask:0xf bank_mask:0xf\n"
    "v_mfma_f32_16x16x32_bf16 v[144:147], a[176:179], v[56:59], v[144:147]\n"
    "ds_read_b64_tr_b16 a[156:157], v22 offset:1544\n"
    "v_add_f32_dpp v208, v208, v208 row_ror:2 row_mask:0xf bank_mask:0xf\n"
    "v_mfma_f32_16x16x32_bf16 v[176:179], a[176:179], v[60:63], v[176:179]\n"
    "ds_read_b64_tr_b16 a[158:159], v22 offset:1800\n"
    "v_add_f32_dpp v209, v209, v209 row_ror:2 row_mask:0xf bank_mask:0xf\n"
    "v_mfma_f32_16x16x32_bf16 v[84:87], a[180:183], v[48:51], v[84:87]\n"
    "ds_read_b64_tr_b16 a[192:193], v23 offset:0\n"
    "v_add_f32_dpp v210, v210, v210 row_ror:2 row_mask:0xf bank_mask:0xf\n"
    "v_mfma_f32_16x16x32_bf16 v[116:119], a[180:183], v[52:55], v[116:119]\n"
    "ds_read_b64_tr_b16 a[194:195], v23 offset:256\n"
    "v_add_f32_dpp v204, v204, v204 row_ror:1 row_mask:0xf bank_mask:0xf\n"
    "v_mfma_f32_16x16x32_bf16 v[148:151], a[180:183], v[56:59], v[148:151]\n"
    "ds_read_b64_tr_b16 a[196:197], v23 offset:8\n"
    "v_add_f32_dpp v205, v205, v205 row_ror:1 row_mask:0xf bank_mask:0xf\n"
    "v_mfma_f32_16x16x32_bf16 v[180:183], a[180:183], v[60:63], v[180:183]\n"
    "ds_read_b64_tr_b16 a[198:199], v23 offset:264\n"
    "v_add_f32_dpp v206, v206, v206 row_ror:1 row_mask:0xf bank_mask:0xf\n"
    "v_mfma_f32_16x16x32_bf16 v[88:91], a[184:187], v[48:51], v[88:91]\n"
    "s_add_u32 m0, s70, 32768\n"
    "s_add_u32 s73, s76, 1576960\n"
    "buffer_load_dwordx4 v19, s[36:39], s73 offen offset:0 lds\n"
    "v_mfma_f32_16x16x32_bf16 v[120:123], a[184:187], v[52:55], v[120:123]\n"
    "v_add_f32_dpp v207, v207, v207 row_ror:1 row_mask:0xf bank_mask:0xf\n"
    "v_mfma_f32_16x16x32_bf16 v[152:155], a[184:187], v[56:59], v[152:155]\n"
    "v_add_f32_dpp v208, v208, v208 row_ror:1 row_mask:0xf bank_mask:0xf\n"
    "v_mfma_f32_16x16x32_bf16 v[184:187], a[184:187], v[60:63], v[184:187]\n"
    "s_add_u32 s73, s76, 1576064\n"
    "buffer_load_dwordx4 v19, s[36:39], s73 offen offset:1024 lds\n"
    "v_mfma_f32_16x16x32_bf16 v[92:95], a[188:191], v[48:51], v[92:95]\n"
    "v_add_f32_dpp v209, v209, v209 row_ror:1 row_mask:0xf bank_mask:0xf\n"
    "v_mfma_f32_16x16x32_bf16 v[124:127], a[188:191], v[52:55], v[124:127]\n"
    "v_add_f32_dpp v210, v210, v210 row_ror:1 row_mask:0xf bank_mask:0xf\n"
    "v_mfma_f32_16x16x32_bf16 v[156:159], a[188:191], v[56:59], v[156:159]\n"
    "s_add_u32 s73, s76, 1640448\n"
    "buffer_load_dwordx4 v19, s[36:39], s73 offen offset:2048 lds\n"
    "v_mfma_f32_16x16x32_bf16 v[188:191], a[188:191], v[60:63], v[188:191]\n"
    "v_add_f32_dpp v204, v204, v204 row_bcast:15 row_mask:0xa bank_mask:0xf\n"
    "v_mfma_f32_16x16x32_bf16 a[208:211], a[232:235], a[200:203], a[208:211]\n"
    "v_add_f32_dpp v205, v205, v205 row_bcast:15 row_mask:0xa bank_mask:0xf\n"
    "v_mfma_f32_16x16x32_bf16 a[212:215], a[232:235], a[204:207], a[212:215]\n"
    "s_add_u32 s73, s76, 1639552\n"
    "buffer_load_dwordx4 v19, s[36:39], s73 offen offset:3072 lds\n"
    "v_mfma_f32_16x16x32_bf16 a[216:219], a[232:235], v[48:51], a[216:219]\n"
    "v_add_f32_dpp v206, v206, v206 row_bcast:15 row_mask:0xa bank_mask:0xf\n"
    "v_mfma_f32_16x16x32_bf16 a[220:223], a[232:235], v[52:55], a[220:223]\n"
    "v_add_f32_dpp v207, v207, v207 row_bcast:15 row_mask:0xa bank_mask:0xf\n"
    "v_mfma_f32_16x16x32_bf16 a[224:227], a[232:235], v[56:59], a[224:227]\n"
    "s_add_u32 m0, s82, 0\n"
    "s_add_u32 s73, s77, 384\n"
    "buffer_load_dwordx4 v20, s[32:35], s73 offen offset:0 lds\n"
    "v_mfma_f32_16x16x32_bf16 a[228:231], a[232:235], v[60:63], a[228:231]\n"
    "v_add_f32_dpp v208, v208, v208 row_bcast:15 row_mask:0xa bank_mask:0xf\n"
    "v_mfma_f32_16x16x32_bf16 a[236:239], v[48:51], v[48:51], a[236:239]\n"
    "v_add_f32_dpp v209, v209, v209 row_bcast:15 row_mask:0xa bank_mask:0xf\n"
    "v_mfma_f32_16x16x32_bf16 a[240:243], v[52:55], v[52:55], a[240:243]\n"
    "s_add_u32 s73, s77, 7552\n"
    "buffer_load_dwordx4 v21, s[32:35], s73 offen offset:1024 lds\n"
    "v_mfma_f32_16x16x32_bf16 a[244:247], v[56:59], v[56:59], a[244:247]\n"
    "v_add_f32_dpp v210, v210, v210 row_bcast:15 row_mask:0xa bank_mask:0xf\n"
    "v_mfma_f32_16x16x32_bf16 a[248:251], v[60:63], v[60:63], a[248:251]\n"
    "v_add_f32_dpp v204, v204, v204 row_bcast:31 row_mask:0xc bank_mask:0xf\n"
    "s_waitcnt lgkmcnt(0)\n"
    "v_mfma_f32_16x16x32_bf16 v[64:67], a[128:131], v[32:35], v[64:67]\n"
    "ds_read_b128 v[48:51], v24 offset:512\n"
    "v_add_f32_dpp v205, v205, v205 row_bcast:31 row_mask:0xc bank_mask:0xf\n"
    "v_mfma_f32_16x16x32_bf16 v[96:99], a[128:131], v[36:39], v[96:99]\n"
    "ds_read_b128 v[52:55], v24 offset:2560\n"
    "v_add_f32_dpp v206, v206, v206 row_bcast:31 row_mask:0xc bank_mask:0xf\n"
    "v_mfma_f32_16x16x32_bf16 v[128:131], a[128:131], v[40:43], v[128:131]\n"
    "ds_read_b128 v[56:59], v24 offset:4608\n"
    "v_add_f32_dpp v207, v207, v207 row_bcast:31 row_mask:0xc bank_mask:0xf\n"
    "v_mfma_f32_16x16x32_bf16 v[160:163], a[128:131], v[44:47], v[160:163]\n"
    "ds_read_b128 v[60:63], v24 offset:6656\n"
    "v_add_f32_dpp v208, v208, v208 row_bcast:31 row_mask:0xc bank_mask:0xf\n"
    "v_mfma_f32_16x16x32_bf16 v[68:71], a[132:135], v[32:35], v[68:71]\n"
    "ds_read_b64_tr_b16 a[160:161], v22 offset:8192\n"
    "v_add_f32_dpp v209, v209, v209 row_bcast:31 row_mask:0xc bank_mask:0xf\n"
    "v_mfma_f32_16x16x32_bf16 v[100:103], a[132:135], v[36:39], v[100:103]\n"
    "ds_read_b64_tr_b16 a[162:163], v22 offset:8448\n"
    "v_add_f32_dpp v210, v210, v210 row_bcast:31 row_mask:0xc bank_mask:0xf\n"
    "v_mfma_f32_16x16x32_bf16 v[132:135], a[132:135], v[40:43], v[132:135]\n"
    "ds_read_b64_tr_b16 a[164:165], v22 offset:8200\n"
    "v_mul_f32 v212, 0x3b000000, v204\n"
    "v_mfma_f32_16x16x32_bf16 v[164:167], a[132:135], v[44:47], v[164:167]\n"
    "ds_read_b64_tr_b16 a[166:167], v22 offset:8456\n"
    "v_mul_f32 v213, 0x3b000000, v205\n"
    "v_mfma_f32_16x16x32_bf16 v[72:75], a[136:139], v[32:35], v[72:75]\n"
    "ds_read_b64_tr_b16 a[168:169], v22 offset:8704\n"
    "v_fma_f32 v213, -v212, v212, v213\n"
    "v_mfma_f32_16x16x32_bf16 v[104:107], a[136:139], v[36:39], v[104:107]\n"
    "ds_read_b64_tr_b16 a[170:171], v22 offset:8960\n"
    "v_max_f32 v213, 0, v213\n"
    "v_mfma_f32_16x16x32_bf16 v[136:139], a[136:139], v[40:43], v[136:139]\n"
    "ds_read_b64_tr_b16 a[172:173], v22 offset:8712\n"
    "v_cvt_pk_bf16_f32 v214, v212, v213\n"
    "v_mfma_f32_16x16x32_bf16 v[168:171], a[136:139], v[44:47], v[168:171]\n"
    "ds_read_b64_tr_b16 a[174:175], v22 offset:8968\n"
    "v_lshlrev_b32 v212, 16, v214\n"
    "v_mfma_f32_16x16x32_bf16 v[76:79], a[140:143], v[32:35], v[76:79]\n"
    "ds_read_b64_tr_b16 a[176:177], v22 offset:9216\n"
    "v_and_b32 v213, 0xffff0000, v214\n"
    "v_mfma_f32_16x16x32_bf16 v[108:111], a[140:143], v[36:39], v[108:111]\n"
    "ds_read_b64_tr_b16 a[178:179], v22 offset:9472\n"
    "v_add_f32 v213, 0x3727c5ac, v213\n"
    "v_mfma_f32_16x16x32_bf16 v[140:143], a[140:143], v[40:43], v[140:143]\n"
    "ds_read_b64_tr_b16 a[180:181], v22 offset:9224\n"
    "v_cvt_pk_bf16_f32 v213, v213, v213\n"
    "v_mfma_f32_16x16x32_bf16 v[172:175], a[140:143], v[44:47], v[172:175]\n"
    "ds_read_b64_tr_b16 a[182:183], v22 offset:9480\n"
    "v_lshlrev_b32 v213, 16, v213\n"
    "v_mfma_f32_16x16x32_bf16 v[80:83], a[144:147], v[32:35], v[80:83]\n"
    "ds_read_b64_tr_b16 a[184:185], v22 offset:9728\n"
    "v_sqrt_f32 v213, v213\n"
    "v_mfma_f32_16x16x32_bf16 v[112:115], a[144:147], v[36:39], v[112:115]\n"
    "ds_read_b64_tr_b16 a[186:187], v22 offset:9984\n"
    "s_nop 0\n"
    "v_mfma_f32_16x16x32_bf16 v[144:147], a[144:147], v[40:43], v[144:147]\n"
    "ds_read_b64_tr_b16 a[188:189], v22 offset:9736\n"
    "v_cvt_pk_bf16_f32 v213, v213, v213\n"
    "v_mfma_f32_16x16x32_bf16 v[176:179], a[144:147], v[44:47], v[176:179]\n"
    "ds_read_b64_tr_b16 a[190:191], v22 offset:9992\n"
    "v_lshlrev_b32 v213, 16, v213\n"
    "v_mfma_f32_16x16x32_bf16 v[84:87], a[148:151], v[32:35], v[84:87]\n"
    "ds_read_b64_tr_b16 a[200:201], v23 offset:8192\n"
    "v_rcp_f32 v214, v213\n"
    "v_mfma_f32_16x16x32_bf16 v[116:119], a[148:151], v[36:39], v[116:119]\n"
    "ds_read_b64_tr_b16 a[202:203], v23 offset:8448\n"
    "s_nop 0\n"
    "v_mfma_f32_16x16x32_bf16 v[148:151], a[148:151], v[40:43], v[148:151]\n"
    "ds_read_b64_tr_b16 a[204:205], v23 offset:8200\n"
    "v_fma_f32 v215, -v212, v210, v206\n"
    "v_mfma_f32_16x16x32_bf16 v[180:183], a[148:151], v[44:47], v[180:183]\n"
    "ds_read_b64_tr_b16 a[206:207], v23 offset:8456\n"
    "v_mul_f32 v220, v215, v214\n"
    "v_mfma_f32_16x16x32_bf16 v[88:91], a[152:155], v[32:35], v[88:91]\n"
    "s_add_u32 s73, s77, 14720\n"
    "buffer_load_dwordx4 v20, s[32:35], s73 offen offset:2048 lds\n"
    "v_mfma_f32_16x16x32_bf16 v[120:123], a[152:155], v[36:39], v[120:123]\n"
    "v_mul_f32 v216, 0x3b000000, v207\n"
    "v_mfma_f32_16x16x32_bf16 v[152:155], a[152:155], v[40:43], v[152:155]\n"
    "v_mul_f32 v217, 0x3b000000, v208\n"
    "v_mfma_f32_16x16x32_bf16 v[184:187], a[152:155], v[44:47], v[184:187]\n"
    "s_add_u32 s73, s77, 21888\n"
    "buffer_load_dwordx4 v21, s[32:35], s73 offen offset:3072 lds\n"
    "v_mfma_f32_16x16x32_bf16 v[92:95], a[156:159], v[32:35], v[92:95]\n"
    "v_fma_f32 v217, -v216, v216, v217\n"
    "v_mfma_f32_16x16x32_bf16 v[124:127], a[156:159], v[36:39], v[124:127]\n"
    "v_max_f32 v217, 0, v217\n"
    "v_mfma_f32_16x16x32_bf16 v[156:159], a[156:159], v[40:43], v[156:159]\n"
    "s_add_u32 m0, s82, 4096\n"
    "s_add_u32 s73, s77, 33152\n"
    "buffer_load_dwordx4 v20, s[32:35], s73 offen offset:0 lds\n"
    "v_mfma_f32_16x16x32_bf16 v[188:191], a[156:159], v[44:47], v[188:191]\n"
    "v_cvt_pk_bf16_f32 v218, v216, v217\n"
    "v_mfma_f32_16x16x32_bf16 a[208:211], a[232:235], a[192:195], a[208:211]\n"
    "v_lshlrev_b32 v216, 16, v218\n"
    "v_mfma_f32_16x16x32_bf16 a[212:215], a[232:235], a[196:199], a[212:215]\n"
    "s_add_u32 s73, s77, 40320\n"
    "buffer_load_dwordx4 v21, s[32:35], s73 offen offset:1024 lds\n"
    "v_mfma_f32_16x16x32_bf16 a[216:219], a[232:235], v[32:35], a[216:219]\n"
    "v_and_b32 v217, 0xffff0000, v218\n"
    "v_mfma_f32_16x16x32_bf16 a[220:223], a[232:235], v[36:39], a[220:223]\n"
    "v_add_f32 v217, 0x3727c5ac, v217\n"
    "v_mfma_f32_16x16x32_bf16 a[224:227], a[232:235], v[40:43], a[224:227]\n"
    "s_add_u32 s73, s77, 47488\n"
    "buffer_load_dwordx4 v20, s[32:35], s73 offen offset:2048 lds\n"
    "v_mfma_f32_16x16x32_bf16 a[228:231], a[232:235], v[44:47], a[228:231]\n"
    "v_cvt_pk_bf16_f32 v217, v217, v217\n"
    "v_mfma_f32_16x16x32_bf16 a[236:239], v[32:35], v[32:35], a[236:239]\n"
    "v_lshlrev_b32 v217, 16, v217\n"
    "v_mfma_f32_16x16x32_bf16 a[240:243], v[36:39], v[36:39], a[240:243]\n"
    "s_add_u32 s73, s77, 54656\n"
    "buffer_load_dwordx4 v21, s[32:35], s73 offen offset:3072 lds\n"
    "v_mfma_f32_16x16x32_bf16 a[244:247], v[40:43], v[40:43], a[244:247]\n"
    "v_sqrt_f32 v217, v217\n"
    "v_mfma_f32_16x16x32_bf16 a[248:251], v[44:47], v[44:47], a[248:251]\n"
    "s_nop 0\n"
    "s_waitcnt lgkmcnt(0)\n"
    "s_waitcnt vmcnt(16)\n"
    "s_barrier\n"
    "v_add_u32 v22, 81920, v22\n"
    "v_add_u32 v23, 81920, v23\n"
    "s_sub_u32 s75, s78, s83\n"
    "v_add_u32 v24, s75, v24\n"
    "v_mfma_f32_16x16x32_bf16 v[64:67], a[160:163], v[48:51], v[64:67]\n"
    "ds_read_b128 v[32:35], v24 offset:0\n"
    "v_cvt_pk_bf16_f32 v217, v217, v217\n"
    "v_mfma_f32_16x16x32_bf16 v[96:99], a[160:163], v[52:55], v[96:99]\n"
    "ds_read_b128 v[36:39], v24 offset:2048\n"
    "v_lshlrev_b32 v217, 16, v217\n"
    "v_mfma_f32_16x16x32_bf16 v[128:131], a[160:163], v[56:59], v[128:131]\n"
    "ds_read_b128 v[40:43], v24 offset:4096\n"
    "v_rcp_f32 v218, v217\n"
    "v_mfma_f32_16x16x32_bf16 v[160:163], a[160:163], v[60:63], v[160:163]\n"
    "ds_read_b128 v[44:47], v24 offset:6144\n"
    "s_nop 0\n"
    "v_mfma_f32_16x16x32_bf16 v[68:71], a[164:167], v[48:51], v[68:71]\n"
    "ds_read_b64_tr_b16 a[128:129], v22 offset:0\n"
    "v_fma_f32 v219, -v216, v210, v209\n"
    "v_mfma_f32_16x16x32_bf16 v[100:103], a[164:167], v[52:55], v[100:103]\n"
    "ds_read_b64_tr_b16 a[130:131], v22 offset:256\n"
    "v_mul_f32 v221, v219, v218\n"
    "v_mfma_f32_16x16x32_bf16 v[132:135], a[164:167], v[56:59], v[132:135]\n"
    "ds_read_b64_tr_b16 a[132:133], v22 offset:8\n"
    "v_mov_b32 v222, 0\n"
    "s_lshl_b32 s75, %7, 2\n"
    "s_add_u32 s75, s75, s64\n"
    "s_lshl_b32 s75, s75, 2\n"
    "s_add_u32 s75, s75, 24576\n"
    "v_mov_b32 v214, v216\n"
    "v_mov_b32 v215, v217\n"
    "s_lshl_b32 s73, s81, 3\n"
    "s_lshl_b32 s74, s81, 2\n"
    "s_add_u32 s74, s74, 16384\n"
    "s_mov_b64 s[84:85], exec\n"
    "s_mov_b32 exec_lo, 0\n"
    "s_brev_b32 exec_hi, 1\n"
    "buffer_store_dwordx4 v[212:215], off, s[60:63], s73\n"
    "buffer_store_dwordx2 v[220:221], off, s[60:63], s74\n"
    "buffer_store_dword v222, off, s[60:63], s75\n"
    "s_mov_b64 exec, s[84:85]\n"
    "v_mfma_f32_16x16x32_bf16 v[164:167], a[164:167], v[60:63], v[164:167]\n"
    "ds_read_b64_tr_b16 a[134:135], v22 offset:264\n"
    "v_accvgpr_write_b32 a0, 0\n"
    "v_mfma_f32_16x16x32_bf16 v[72:75], a[168:171], v[48:51], v[72:75]\n"
    "ds_read_b64_tr_b16 a[136:137], v22 offset:512\n"
    "v_accvgpr_write_b32 a1, 0\n"
    "v_mfma_f32_16x16x32_bf16 v[104:107], a[168:171], v[52:55], v[104:107]\n"
    "ds_read_b64_tr_b16 a[138:139], v22 offset:768\n"
    "v_accvgpr_write_b32 a2, 0\n"
    "v_mfma_f32_16x16x32_bf16 v[136:139], a[168:171], v[56:59], v[136:139]\n"
    "ds_read_b64_tr_b16 a[140:141], v22 offset:520\n"
    "v_accvgpr_write_b32 a3, 0\n"
    "v_mfma_f32_16x16x32_bf16 v[168:171], a[168:171], v[60:63], v[168:171]\n"
    "ds_read_b64_tr_b16 a[142:143], v22 offset:776\n"
    "v_accvgpr_write_b32 a4, 0\n"
    "v_mfma_f32_16x16x32_bf16 v[76:79], a[172:175], v[48:51], v[76:79]\n"
    "ds_read_b64_tr_b16 a[144:145], v22 offset:1024\n"
    "v_accvgpr_write_b32 a5, 0\n"
    "v_mfma_f32_16x16x32_bf16 v[108:111], a[172:175], v[52:55], v[108:111]\n"
    "ds_read_b64_tr_b16 a[146:147], v22 offset:1280\n"
    "v_accvgpr_write_b32 a6, 0\n"
    "v_mfma_f32_16x16x32_bf16 v[140:143], a[172:175], v[56:59], v[140:143]\n"
    "ds_read_b64_tr_b16 a[148:149], v22 offset:1032\n"
    "v_accvgpr_write_b32 a7, 0\n"
    "v_mfma_f32_16x16x32_bf16 v[172:175], a[172:175], v[60:63], v[172:175]\n"
    "ds_read_b64_tr_b16 a[150:151], v22 offset:1288\n"
    "v_accvgpr_write_b32 a8, 0\n"
    "v_mfma_f32_16x16x32_bf16 v[80:83], a[176:179], v[48:51], v[80:83]\n"
    "ds_read_b64_tr_b16 a[152:153], v22 offset:1536\n"
    "v_accvgpr_write_b32 a9, 0\n"
    "v_mfma_f32_16x16x32_bf16 v[112:115], a[176:179], v[52:55], v[112:115]\n"
    "ds_read_b64_tr_b16 a[154:155], v22 offset:1792\n"
    "v_accvgpr_write_b32 a10, 0\n"
    "v_mfma_f32_16x16x32_bf16 v[144:147], a[176:179], v[56:59], v[144:147]\n"
    "ds_read_b64_tr_b16 a[156:157], v22 offset:1544\n"
    "v_accvgpr_write_b32 a11, 0\n"
    "v_mfma_f32_16x16x32_bf16 v[176:179], a[176:179], v[60:63], v[176:179]\n"
    "ds_read_b64_tr_b16 a[158:159], v22 offset:1800\n"
    "v_accvgpr_write_b32 a12, 0\n"
    "v_mfma_f32_16x16x32_bf16 v[84:87], a[180:183], v[48:51], v[84:87]\n"
    "ds_read_b64_tr_b16 a[192:193], v23 offset:0\n"
    "v_accvgpr_write_b32 a13, 0\n"
    "v_mfma_f32_16x16x32_bf16 v[116:119], a[180:183], v[52:55], v[116:119]\n"
    "ds_read_b64_tr_b16 a[194:195], v23 offset:256\n"
    "v_accvgpr_write_b32 a14, 0\n"
    "v_mfma_f32_16x16x32_bf16 v[148:151], a[180:183], v[56:59], v[148:151]\n"
    "ds_read_b64_tr_b16 a[196:197], v23 offset:8\n"
    "v_accvgpr_write_b32 a15, 0\n"
    "v_mfma_f32_16x16x32_bf16 v[180:183], a[180:183], v[60:63], v[180:183]\n"
    "ds_read_b64_tr_b16 a[198:199], v23 offset:264\n"
    "v_accvgpr_write_b32 a16, 0\n"
    "v_mfma_f32_16x16x32_bf16 v[88:91], a[184:187], v[48:51], v[88:91]\n"
    "s_add_u32 m0, s70, 49152\n"
    "s_add_u32 s73, s76, 2101248\n"
    "buffer_load_dwordx4 v19, s[36:39], s73 offen offset:0 lds\n"
    "v_mfma_f32_16x16x32_bf16 v[120:123], a[184:187], v[52:55], v[120:123]\n"
    "v_accvgpr_write_b32 a17, 0\n"
    "v_mfma_f32_16x16x32_bf16 v[152:155], a[184:187], v[56:59], v[152:155]\n"
    "v_accvgpr_write_b32 a18, 0\n"
    "v_mfma_f32_16x16x32_bf16 v[184:187], a[184:187], v[60:63], v[184:187]\n"
    "s_add_u32 s73, s76, 2100352\n"
    "buffer_load_dwordx4 v19, s[36:39], s73 offen offset:1024 lds\n"
    "v_mfma_f32_16x16x32_bf16 v[92:95], a[188:191], v[48:51], v[92:95]\n"
    "v_accvgpr_write_b32 a19, 0\n"
    "v_mfma_f32_16x16x32_bf16 v[124:127], a[188:191], v[52:55], v[124:127]\n"
    "v_accvgpr_write_b32 a20, 0\n"
    "v_mfma_f32_16x16x32_bf16 v[156:159], a[188:191], v[56:59], v[156:159]\n"
    "s_add_u32 s73, s76, 2164736\n"
    "buffer_load_dwordx4 v19, s[36:39], s73 offen offset:2048 lds\n"
    "v_mfma_f32_16x16x32_bf16 v[188:191], a[188:191], v[60:63], v[188:191]\n"
    "v_accvgpr_write_b32 a21, 0\n"
    "v_mfma_f32_16x16x32_bf16 a[208:211], a[232:235], a[200:203], a[208:211]\n"
    "v_accvgpr_write_b32 a22, 0\n"
    "v_mfma_f32_16x16x32_bf16 a[212:215], a[232:235], a[204:207], a[212:215]\n"
    "s_add_u32 s73, s76, 2163840\n"
    "buffer_load_dwordx4 v19, s[36:39], s73 offen offset:3072 lds\n"
    "v_mfma_f32_16x16x32_bf16 a[216:219], a[232:235], v[48:51], a[216:219]\n"
    "v_accvgpr_write_b32 a23, 0\n"
    "v_mfma_f32_16x16x32_bf16 a[220:223], a[232:235], v[52:55], a[220:223]\n"
    "v_accvgpr_write_b32 a24, 0\n"
    "v_mfma_f32_16x16x32_bf16 a[224:227], a[232:235], v[56:59], a[224:227]\n"
    "s_add_u32 m0, s83, 0\n"
    "s_add_u32 s73, s77, 512\n"
    "buffer_load_dwordx4 v20, s[32:35], s73 offen offset:0 lds\n"
    "v_mfma_f32_16x16x32_bf16 a[228:231], a[232:235], v[60:63], a[228:231]\n"
    "v_accvgpr_write_b32 a25, 0\n"
    "v_mfma_f32_16x16x32_bf16 a[236:239], v[48:51], v[48:51], a[236:239]\n"
    "v_accvgpr_write_b32 a26, 0\n"
    "v_mfma_f32_16x16x32_bf16 a[240:243], v[52:55], v[52:55], a[240:243]\n"
    "s_add_u32 s73, s77, 7680\n"
    "buffer_load_dwordx4 v21, s[32:35], s73 offen offset:1024 lds\n"
    "v_mfma_f32_16x16x32_bf16 a[244:247], v[56:59], v[56:59], a[244:247]\n"
    "v_accvgpr_write_b32 a27, 0\n"
    "v_mfma_f32_16x16x32_bf16 a[248:251], v[60:63], v[60:63], a[248:251]\n"
    "v_accvgpr_write_b32 a28, 0\n"
    "s_waitcnt lgkmcnt(0)\n"
    "v_mfma_f32_16x16x32_bf16 v[64:67], a[128:131], v[32:35], v[64:67]\n"
    "ds_read_b128 v[48:51], v24 offset:512\n"
    "v_accvgpr_write_b32 a29, 0\n"
    "v_mfma_f32_16x16x32_bf16 v[96:99], a[128:131], v[36:39], v[96:99]\n"
    "ds_read_b128 v[52:55], v24 offset:2560\n"
    "v_accvgpr_write_b32 a30, 0\n"
    "v_mfma_f32_16x16x32_bf16 v[128:131], a[128:131], v[40:43], v[128:131]\n"
    "ds_read_b128 v[56:59], v24 offset:4608\n"
    "v_accvgpr_write_b32 a31, 0\n"
    "v_mfma_f32_16x16x32_bf16 v[160:163], a[128:131], v[44:47], v[160:163]\n"
    "ds_read_b128 v[60:63], v24 offset:6656\n"
    "v_accvgpr_write_b32 a32, 0\n"
    "v_mfma_f32_16x16x32_bf16 v[68:71], a[132:135], v[32:35], v[68:71]\n"
    "ds_read_b64_tr_b16 a[160:161], v22 offset:8192\n"
    "v_accvgpr_write_b32 a33, 0\n"
    "v_mfma_f32_16x16x32_bf16 v[100:103], a[132:135], v[36:39], v[100:103]\n"
    "ds_read_b64_tr_b16 a[162:163], v22 offset:8448\n"
    "v_accvgpr_write_b32 a34, 0\n"
    "v_mfma_f32_16x16x32_bf16 v[132:135], a[132:135], v[40:43], v[132:135]\n"
    "ds_read_b64_tr_b16 a[164:165], v22 offset:8200\n"
    "v_accvgpr_write_b32 a35, 0\n"
    "v_mfma_f32_16x16x32_bf16 v[164:167], a[132:135], v[44:47], v[164:167]\n"
    "ds_read_b64_tr_b16 a[166:167], v22 offset:8456\n"
    "v_accvgpr_write_b32 a36, 0\n"
    "v_mfma_f32_16x16x32_bf16 v[72:75], a[136:139], v[32:35], v[72:75]\n"
    "ds_read_b64_tr_b16 a[168:169], v22 offset:8704\n"
    "v_accvgpr_write_b32 a37, 0\n"
    "v_mfma_f32_16x16x32_bf16 v[104:107], a[136:139], v[36:39], v[104:107]\n"
    "ds_read_b64_tr_b16 a[170:171], v22 offset:8960\n"
    "v_accvgpr_write_b32 a38, 0\n"
    "v_mfma_f32_16x16x32_bf16 v[136:139], a[136:139], v[40:43], v[136:139]\n"
    "ds_read_b64_tr_b16 a[172:173], v22 offset:8712\n"
    "v_accvgpr_write_b32 a39, 0\n"
    "v_mfma_f32_16x16x32_bf16 v[168:171], a[136:139], v[44:47], v[168:171]\n"
    "ds_read_b64_tr_b16 a[174:175], v22 offset:8968\n"
    "v_accvgpr_write_b32 a40, 0\n"
    "v_mfma_f32_16x16x32_bf16 v[76:79], a[140:143], v[32:35], v[76:79]\n"
    "ds_read_b64_tr_b16 a[176:177], v22 offset:9216\n"
    "v_accvgpr_write_b32 a41, 0\n"
    "v_mfma_f32_16x16x32_bf16 v[108:111], a[140:143], v[36:39], v[108:111]\n"
    "ds_read_b64_tr_b16 a[178:179], v22 offset:9472\n"
    "v_accvgpr_write_b32 a42, 0\n"
    "v_mfma_f32_16x16x32_bf16 v[140:143], a[140:143], v[40:43], v[140:143]\n"
    "ds_read_b64_tr_b16 a[180:181], v22 offset:9224\n"
    "v_accvgpr_write_b32 a43, 0\n"
    "v_mfma_f32_16x16x32_bf16 v[172:175], a[140:143], v[44:47], v[172:175]\n"
    "ds_read_b64_tr_b16 a[182:183], v22 offset:9480\n"
    "v_accvgpr_write_b32 a44, 0\n"
    "v_mfma_f32_16x16x32_bf16 v[80:83], a[144:147], v[32:35], v[80:83]\n"
    "ds_read_b64_tr_b16 a[184:185], v22 offset:9728\n"
    "v_accvgpr_write_b32 a45, 0\n"
    "v_mfma_f32_16x16x32_bf16 v[112:115], a[144:147], v[36:39], v[112:115]\n"
    "ds_read_b64_tr_b16 a[186:187], v22 offset:9984\n"
    "v_accvgpr_write_b32 a46, 0\n"
    "v_mfma_f32_16x16x32_bf16 v[144:147], a[144:147], v[40:43], v[144:147]\n"
    "ds_read_b64_tr_b16 a[188:189], v22 offset:9736\n"
    "v_accvgpr_write_b32 a47, 0\n"
    "v_mfma_f32_16x16x32_bf16 v[176:179], a[144:147], v[44:47], v[176:179]\n"
    "ds_read_b64_tr_b16 a[190:191], v22 offset:9992\n"
    "v_accvgpr_write_b32 a48, 0\n"
    "v_mfma_f32_16x16x32_bf16 v[84:87], a[148:151], v[32:35], v[84:87]\n"
    "ds_read_b64_tr_b16 a[200:201], v23 offset:8192\n"
    "v_accvgpr_write_b32 a49, 0\n"
    "v_mfma_f32_16x16x32_bf16 v[116:119], a[148:151], v[36:39], v[116:119]\n"
    "ds_read_b64_tr_b16 a[202:203], v23 offset:8448\n"
    "v_accvgpr_write_b32 a50, 0\n"
    "v_mfma_f32_16x16x32_bf16 v[148:151], a[148:151], v[40:43], v[148:151]\n"
    "ds_read_b64_tr_b16 a[204:205], v23 offset:8200\n"
    "v_accvgpr_write_b32 a51, 0\n"
    "v_mfma_f32_16x16x32_bf16 v[180:183], a[148:151], v[44:47], v[180:183]\n"
    "ds_read_b64_tr_b16 a[206:207], v23 offset:8456\n"
    "v_accvgpr_write_b32 a52, 0\n"
    "v_mfma_f32_16x16x32_bf16 v[88:91], a[152:155], v[32:35], v[88:91]\n"
    "s_add_u32 s73, s77, 14848\n"
    "buffer_load_dwordx4 v20, s[32:35], s73 offen offset:2048 lds\n"
    "v_mfma_f32_16x16x32_bf16 v[120:123], a[152:155], v[36:39], v[120:123]\n"
    "v_accvgpr_write_b32 a53, 0\n"
    "v_mfma_f32_16x16x32_bf16 v[152:155], a[152:155], v[40:43], v[152:155]\n"
    "v_accvgpr_write_b32 a54, 0\n"
    "v_mfma_f32_16x16x32_bf16 v[184:187], a[152:155], v[44:47], v[184:187]\n"
    "s_add_u32 s73, s77, 22016\n"
    "buffer_load_dwordx4 v21, s[32:35], s73 offen offset:3072 lds\n"
    "v_mfma_f32_16x16x32_bf16 v[92:95], a[156:159], v[32:35], v[92:95]\n"
    "v_accvgpr_write_b32 a55, 0\n"
    "v_mfma_f32_16x16x32_bf16 v[124:127], a[156:159], v[36:39], v[124:127]\n"
    "v_accvgpr_write_b32 a56, 0\n"
    "v_mfma_f32_16x16x32_bf16 v[156:159], a[156:159], v[40:43], v[156:159]\n"
    "s_add_u32 m0, s83, 4096\n"
    "s_add_u32 s73, s77, 33280\n"
    "buffer_load_dwordx4 v20, s[32:35], s73 offen offset:0 lds\n"
    "v_mfma_f32_16x16x32_bf16 v[188:191], a[156:159], v[44:47], v[188:191]\n"
    "v_accvgpr_write_b32 a57, 0\n"
    "v_mfma_f32_16x16x32_bf16 a[208:211], a[232:235], a[192:195], a[208:211]\n"
    "v_accvgpr_write_b32 a58, 0\n"
    "v_mfma_f32_16x16x32_bf16 a[212:215], a[232:235], a[196:199], a[212:215]\n"
    "s_add_u32 s73, s77, 40448\n"
    "buffer_load_dwordx4 v21, s[32:35], s73 offen offset:1024 lds\n"
    "v_mfma_f32_16x16x32_bf16 a[216:219], a[232:235], v[32:35], a[216:219]\n"
    "v_accvgpr_write_b32 a59, 0\n"
    "v_mfma_f32_16x16x32_bf16 a[220:223], a[232:235], v[36:39], a[220:223]\n"
    "v_accvgpr_write_b32 a60, 0\n"
    "v_mfma_f32_16x16x32_bf16 a[224:227], a[232:235], v[40:43], a[224:227]\n"
    "s_add_u32 s73, s77, 47616\n"
    "buffer_load_dwordx4 v20, s[32:35], s73 offen offset:2048 lds\n"
    "v_mfma_f32_16x16x32_bf16 a[228:231], a[232:235], v[44:47], a[228:231]\n"
    "v_accvgpr_write_b32 a61, 0\n"
    "v_mfma_f32_16x16x32_bf16 a[236:239], v[32:35], v[32:35], a[236:239]\n"
    "v_accvgpr_write_b32 a62, 0\n"
    "v_mfma_f32_16x16x32_bf16 a[240:243], v[36:39], v[36:39], a[240:243]\n"
    "s_add_u32 s73, s77, 54784\n"
    "buffer_load_dwordx4 v21, s[32:35], s73 offen offset:3072 lds\n"
    "v_mfma_f32_16x16x32_bf16 a[244:247], v[40:43], v[40:43], a[244:247]\n"
    "v_accvgpr_write_b32 a63, 0\n"
    "v_mfma_f32_16x16x32_bf16 a[248:251], v[44:47], v[44:47], a[248:251]\n"
    "v_accvgpr_write_b32 a64, 0\n"
    "s_waitcnt lgkmcnt(0)\n"
    "s_waitcnt vmcnt(15)\n"
    "s_barrier\n"
    "v_add_u32 v22, 4294868992, v22\n"
    "v_add_u32 v23, 4294868992, v23\n"
    "s_sub_u32 s75, s82, s78\n"
    "v_add_u32 v24, s75, v24\n"
    "v_mfma_f32_16x16x32_bf16 v[64:67], a[160:163], v[48:51], v[64:67]\n"
    "ds_read_b128 v[32:35], v24 offset:0\n"
    "v_accvgpr_write_b32 a65, 0\n"
    "v_mfma_f32_16x16x32_bf16 v[96:99], a[160:163], v[52:55], v[96:99]\n"
    "ds_read_b128 v[36:39], v24 offset:2048\n"
    "v_accvgpr_write_b32 a66, 0\n"
    "v_mfma_f32_16x16x32_bf16 v[128:131], a[160:163], v[56:59], v[128:131]\n"
    "ds_read_b128 v[40:43], v24 offset:4096\n"
    "v_accvgpr_write_b32 a67, 0\n"
    "v_mfma_f32_16x16x32_bf16 v[160:163], a[160:163], v[60:63], v[160:163]\n"
    "ds_read_b128 v[44:47], v24 offset:6144\n"
    "v_accvgpr_write_b32 a68, 0\n"
    "v_mfma_f32_16x16x32_bf16 v[68:71], a[164:167], v[48:51], v[68:71]\n"
    "ds_read_b64_tr_b16 a[128:129], v22 offset:0\n"
    "v_accvgpr_write_b32 a69, 0\n"
    "v_mfma_f32_16x16x32_bf16 v[100:103], a[164:167], v[52:55], v[100:103]\n"
    "ds_read_b64_tr_b16 a[130:131], v22 offset:256\n"
    "v_accvgpr_write_b32 a70, 0\n"
    "v_mfma_f32_16x16x32_bf16 v[132:135], a[164:167], v[56:59], v[132:135]\n"
    "ds_read_b64_tr_b16 a[132:133], v22 offset:8\n"
    "v_accvgpr_write_b32 a71, 0\n"
    "v_mfma_f32_16x16x32_bf16 v[164:167], a[164:167], v[60:63], v[164:167]\n"
    "ds_read_b64_tr_b16 a[134:135], v22 offset:264\n"
    "v_accvgpr_write_b32 a72, 0\n"
    "v_mfma_f32_16x16x32_bf16 v[72:75], a[168:171], v[48:51], v[72:75]\n"
    "ds_read_b64_tr_b16 a[136:137], v22 offset:512\n"
    "v_accvgpr_write_b32 a73, 0\n"
    "v_mfma_f32_16x16x32_bf16 v[104:107], a[168:171], v[52:55], v[104:107]\n"
    "ds_read_b64_tr_b16 a[138:139], v22 offset:768\n"
    "v_accvgpr_write_b32 a74, 0\n"
    "v_mfma_f32_16x16x32_bf16 v[136:139], a[168:171], v[56:59], v[136:139]\n"
    "ds_read_b64_tr_b16 a[140:141], v22 offset:520\n"
    "v_accvgpr_write_b32 a75, 0\n"
    "v_mfma_f32_16x16x32_bf16 v[168:171], a[168:171], v[60:63], v[168:171]\n"
    "ds_read_b64_tr_b16 a[142:143], v22 offset:776\n"
    "v_accvgpr_write_b32 a76, 0\n"
    "v_mfma_f32_16x16x32_bf16 v[76:79], a[172:175], v[48:51], v[76:79]\n"
    "ds_read_b64_tr_b16 a[144:145], v22 offset:1024\n"
    "v_accvgpr_write_b32 a77, 0\n"
    "v_mfma_f32_16x16x32_bf16 v[108:111], a[172:175], v[52:55], v[108:111]\n"
    "ds_read_b64_tr_b16 a[146:147], v22 offset:1280\n"
    "v_accvgpr_write_b32 a78, 0\n"
    "v_mfma_f32_16x16x32_bf16 v[140:143], a[172:175], v[56:59], v[140:143]\n"
    "ds_read_b64_tr_b16 a[148:149], v22 offset:1032\n"
    "v_accvgpr_write_b32 a79, 0\n"
    "v_mfma_f32_16x16x32_bf16 v[172:175], a[172:175], v[60:63], v[172:175]\n"
    "ds_read_b64_tr_b16 a[150:151], v22 offset:1288\n"
    "v_accvgpr_write_b32 a80, 0\n"
    "v_mfma_f32_16x16x32_bf16 v[80:83], a[176:179], v[48:51], v[80:83]\n"
    "ds_read_b64_tr_b16 a[152:153], v22 offset:1536\n"
    "v_accvgpr_write_b32 a81, 0\n"
    "v_mfma_f32_16x16x32_bf16 v[112:115], a[176:179], v[52:55], v[112:115]\n"
    "ds_read_b64_tr_b16 a[154:155], v22 offset:1792\n"
    "v_accvgpr_write_b32 a82, 0\n"
    "v_mfma_f32_16x16x32_bf16 v[144:147], a[176:179], v[56:59], v[144:147]\n"
    "ds_read_b64_tr_b16 a[156:157], v22 offset:1544\n"
    "v_accvgpr_write_b32 a83, 0\n"
    "v_mfma_f32_16x16x32_bf16 v[176:179], a[176:179], v[60:63], v[176:179]\n"
    "ds_read_b64_tr_b16 a[158:159], v22 offset:1800\n"
    "v_accvgpr_write_b32 a84, 0\n"
    "v_mfma_f32_16x16x32_bf16 v[84:87], a[180:183], v[48:51], v[84:87]\n"
    "ds_read_b64_tr_b16 a[192:193], v23 offset:0\n"
    "v_accvgpr_write_b32 a85, 0\n"
    "v_mfma_f32_16x16x32_bf16 v[116:119], a[180:183], v[52:55], v[116:119]\n"
    "ds_read_b64_tr_b16 a[194:195], v23 offset:256\n"
    "v_accvgpr_write_b32 a86, 0\n"
    "v_mfma_f32_16x16x32_bf16 v[148:151], a[180:183], v[56:59], v[148:151]\n"
    "ds_read_b64_tr_b16 a[196:197], v23 offset:8\n"
    "v_accvgpr_write_b32 a87, 0\n"
    "v_mfma_f32_16x16x32_bf16 v[180:183], a[180:183], v[60:63], v[180:183]\n"
    "ds_read_b64_tr_b16 a[198:199], v23 offset:264\n"
    "v_accvgpr_write_b32 a88, 0\n"
    "v_mfma_f32_16x16x32_bf16 v[88:91], a[184:187], v[48:51], v[88:91]\n"
    "s_add_u32 m0, s70, 131072\n"
    "s_add_u32 s73, s76, 2625536\n"
    "buffer_load_dwordx4 v19, s[36:39], s73 offen offset:0 lds\n"
    "v_mfma_f32_16x16x32_bf16 v[120:123], a[184:187], v[52:55], v[120:123]\n"
    "v_accvgpr_write_b32 a89, 0\n"
    "v_mfma_f32_16x16x32_bf16 v[152:155], a[184:187], v[56:59], v[152:155]\n"
    "v_accvgpr_write_b32 a90, 0\n"
    "v_mfma_f32_16x16x32_bf16 v[184:187], a[184:187], v[60:63], v[184:187]\n"
    "s_add_u32 s73, s76, 2624640\n"
    "buffer_load_dwordx4 v19, s[36:39], s73 offen offset:1024 lds\n"
    "v_mfma_f32_16x16x32_bf16 v[92:95], a[188:191], v[48:51], v[92:95]\n"
    "v_accvgpr_write_b32 a91, 0\n"
    "v_mfma_f32_16x16x32_bf16 v[124:127], a[188:191], v[52:55], v[124:127]\n"
    "v_accvgpr_write_b32 a92, 0\n"
    "v_mfma_f32_16x16x32_bf16 v[156:159], a[188:191], v[56:59], v[156:159]\n"
    "s_add_u32 s73, s76, 2689024\n"
    "buffer_load_dwordx4 v19, s[36:39], s73 offen offset:2048 lds\n"
    "v_mfma_f32_16x16x32_bf16 v[188:191], a[188:191], v[60:63], v[188:191]\n"
    "v_accvgpr_write_b32 a93, 0\n"
    "v_mfma_f32_16x16x32_bf16 a[208:211], a[232:235], a[200:203], a[208:211]\n"
    "v_accvgpr_write_b32 a94, 0\n"
    "v_mfma_f32_16x16x32_bf16 a[212:215], a[232:235], a[204:207], a[212:215]\n"
    "s_add_u32 s73, s76, 2688128\n"
    "buffer_load_dwordx4 v19, s[36:39], s73 offen offset:3072 lds\n"
    "v_mfma_f32_16x16x32_bf16 a[216:219], a[232:235], v[48:51], a[216:219]\n"
    "v_accvgpr_write_b32 a95, 0\n"
    "v_mfma_f32_16x16x32_bf16 a[220:223], a[232:235], v[52:55], a[220:223]\n"
    "v_accvgpr_write_b32 a96, 0\n"
    "v_mfma_f32_16x16x32_bf16 a[224:227], a[232:235], v[56:59], a[224:227]\n"
    "s_add_u32 m0, s78, 0\n"
    "s_add_u32 s73, s77, 640\n"
    "buffer_load_dwordx4 v20, s[32:35], s73 offen offset:0 lds\n"
    "v_mfma_f32_16x16x32_bf16 a[228:231], a[232:235], v[60:63], a[228:231]\n"
    "v_accvgpr_write_b32 a97, 0\n"
    "v_mfma_f32_16x16x32_bf16 a[236:239], v[48:51], v[48:51], a[236:239]\n"
    "v_accvgpr_write_b32 a98, 0\n"
    "v_mfma_f32_16x16x32_bf16 a[240:243], v[52:55], v[52:55], a[240:243]\n"
    "s_add_u32 s73, s77, 7808\n"
    "buffer_load_dwordx4 v21, s[32:35], s73 offen offset:1024 lds\n"
    "v_mfma_f32_16x16x32_bf16 a[244:247], v[56:59], v[56:59], a[244:247]\n"
    "v_accvgpr_write_b32 a99, 0\n"
    "v_mfma_f32_16x16x32_bf16 a[248:251], v[60:63], v[60:63], a[248:251]\n"
    "v_accvgpr_write_b32 a100, 0\n"
    "s_waitcnt lgkmcnt(0)\n"
    "v_mfma_f32_16x16x32_bf16 v[64:67], a[128:131], v[32:35], v[64:67]\n"
    "ds_read_b128 v[48:51], v24 offset:512\n"
    "v_accvgpr_write_b32 a101, 0\n"
    "v_mfma_f32_16x16x32_bf16 v[96:99], a[128:131], v[36:39], v[96:99]\n"
    "ds_read_b128 v[52:55], v24 offset:2560\n"
    "v_accvgpr_write_b32 a102, 0\n"
    "v_mfma_f32_16x16x32_bf16 v[128:131], a[128:131], v[40:43], v[128:131]\n"
    "ds_read_b128 v[56:59], v24 offset:4608\n"
    "v_accvgpr_write_b32 a103, 0\n"
    "v_mfma_f32_16x16x32_bf16 v[160:163], a[128:131], v[44:47], v[160:163]\n"
    "ds_read_b128 v[60:63], v24 offset:6656\n"
    "v_accvgpr_write_b32 a104, 0\n"
    "v_mfma_f32_16x16x32_bf16 v[68:71], a[132:135], v[32:35], v[68:71]\n"
    "ds_read_b64_tr_b16 a[160:161], v22 offset:8192\n"
    "v_accvgpr_write_b32 a105, 0\n"
    "v_mfma_f32_16x16x32_bf16 v[100:103], a[132:135], v[36:39], v[100:103]\n"
    "ds_read_b64_tr_b16 a[162:163], v22 offset:8448\n"
    "v_accvgpr_write_b32 a106, 0\n"
    "v_mfma_f32_16x16x32_bf16 v[132:135], a[132:135], v[40:43], v[132:135]\n"
    "ds_read_b64_tr_b16 a[164:165], v22 offset:8200\n"
    "v_accvgpr_write_b32 a107, 0\n"
    "v_mfma_f32_16x16x32_bf16 v[164:167], a[132:135], v[44:47], v[164:167]\n"
    "ds_read_b64_tr_b16 a[166:167], v22 offset:8456\n"
    "v_accvgpr_write_b32 a108, 0\n"
    "v_mfma_f32_16x16x32_bf16 v[72:75], a[136:139], v[32:35], v[72:75]\n"
    "ds_read_b64_tr_b16 a[168:169], v22 offset:8704\n"
    "v_accvgpr_write_b32 a109, 0\n"
    "v_mfma_f32_16x16x32_bf16 v[104:107], a[136:139], v[36:39], v[104:107]\n"
    "ds_read_b64_tr_b16 a[170:171], v22 offset:8960\n"
    "v_accvgpr_write_b32 a110, 0\n"
    "v_mfma_f32_16x16x32_bf16 v[136:139], a[136:139], v[40:43], v[136:139]\n"
    "ds_read_b64_tr_b16 a[172:173], v22 offset:8712\n"
    "v_accvgpr_write_b32 a111, 0\n"
    "v_mfma_f32_16x16x32_bf16 v[168:171], a[136:139], v[44:47], v[168:171]\n"
    "ds_read_b64_tr_b16 a[174:175], v22 offset:8968\n"
    "v_accvgpr_write_b32 a112, 0\n"
    "v_mfma_f32_16x16x32_bf16 v[76:79], a[140:143], v[32:35], v[76:79]\n"
    "ds_read_b64_tr_b16 a[176:177], v22 offset:9216\n"
    "v_accvgpr_write_b32 a113, 0\n"
    "v_mfma_f32_16x16x32_bf16 v[108:111], a[140:143], v[36:39], v[108:111]\n"
    "ds_read_b64_tr_b16 a[178:179], v22 offset:9472\n"
    "v_accvgpr_write_b32 a114, 0\n"
    "v_mfma_f32_16x16x32_bf16 v[140:143], a[140:143], v[40:43], v[140:143]\n"
    "ds_read_b64_tr_b16 a[180:181], v22 offset:9224\n"
    "v_accvgpr_write_b32 a115, 0\n"
    "v_mfma_f32_16x16x32_bf16 v[172:175], a[140:143], v[44:47], v[172:175]\n"
    "ds_read_b64_tr_b16 a[182:183], v22 offset:9480\n"
    "v_accvgpr_write_b32 a116, 0\n"
    "v_mfma_f32_16x16x32_bf16 v[80:83], a[144:147], v[32:35], v[80:83]\n"
    "ds_read_b64_tr_b16 a[184:185], v22 offset:9728\n"
    "v_accvgpr_write_b32 a117, 0\n"
    "v_mfma_f32_16x16x32_bf16 v[112:115], a[144:147], v[36:39], v[112:115]\n"
    "ds_read_b64_tr_b16 a[186:187], v22 offset:9984\n"
    "v_accvgpr_write_b32 a118, 0\n"
    "v_mfma_f32_16x16x32_bf16 v[144:147], a[144:147], v[40:43], v[144:147]\n"
    "ds_read_b64_tr_b16 a[188:189], v22 offset:9736\n"
    "v_accvgpr_write_b32 a119, 0\n"
    "v_mfma_f32_16x16x32_bf16 v[176:179], a[144:147], v[44:47], v[176:179]\n"
    "ds_read_b64_tr_b16 a[190:191], v22 offset:9992\n"
    "v_accvgpr_write_b32 a120, 0\n"
    "v_mfma_f32_16x16x32_bf16 v[84:87], a[148:151], v[32:35], v[84:87]\n"
    "ds_read_b64_tr_b16 a[200:201], v23 offset:8192\n"
    "v_accvgpr_write_b32 a121, 0\n"
    "v_mfma_f32_16x16x32_bf16 v[116:119], a[148:151], v[36:39], v[116:119]\n"
    "ds_read_b64_tr_b16 a[202:203], v23 offset:8448\n"
    "v_accvgpr_write_b32 a122, 0\n"
    "v_mfma_f32_16x16x32_bf16 v[148:151], a[148:151], v[40:43], v[148:151]\n"
    "ds_read_b64_tr_b16 a[204:205], v23 offset:8200\n"
    "v_accvgpr_write_b32 a123, 0\n"
    "v_mfma_f32_16x16x32_bf16 v[180:183], a[148:151], v[44:47], v[180:183]\n"
    "ds_read_b64_tr_b16 a[206:207], v23 offset:8456\n"
    "v_accvgpr_write_b32 a124, 0\n"
    "v_mfma_f32_16x16x32_bf16 v[88:91], a[152:155], v[32:35], v[88:91]\n"
    "s_add_u32 s73, s77, 14976\n"
    "buffer_load_dwordx4 v20, s[32:35], s73 offen offset:2048 lds\n"
    "v_mfma_f32_16x16x32_bf16 v[120:123], a[152:155], v[36:39], v[120:123]\n"
    "v_accvgpr_write_b32 a125, 0\n"
    "v_mfma_f32_16x16x32_bf16 v[152:155], a[152:155], v[40:43], v[152:155]\n"
    "v_accvgpr_write_b32 a126, 0\n"
    "v_mfma_f32_16x16x32_bf16 v[184:187], a[152:155], v[44:47], v[184:187]\n"
    "s_add_u32 s73, s77, 22144\n"
    "buffer_load_dwordx4 v21, s[32:35], s73 offen offset:3072 lds\n"
    "v_mfma_f32_16x16x32_bf16 v[92:95], a[156:159], v[32:35], v[92:95]\n"
    "v_accvgpr_write_b32 a127, 0\n"
    "v_mfma_f32_16x16x32_bf16 v[124:127], a[156:159], v[36:39], v[124:127]\n"
    "v_mfma_f32_16x16x32_bf16 v[156:159], a[156:159], v[40:43], v[156:159]\n"
    "s_add_u32 m0, s78, 4096\n"
    "s_add_u32 s73, s77, 33408\n"
    "buffer_load_dwordx4 v20, s[32:35], s73 offen offset:0 lds\n"
    "v_mfma_f32_16x16x32_bf16 v[188:191], a[156:159], v[44:47], v[188:191]\n"
    "v_mfma_f32_16x16x32_bf16 a[208:211], a[232:235], a[192:195], a[208:211]\n"
    "v_mfma_f32_16x16x32_bf16 a[212:215], a[232:235], a[196:199], a[212:215]\n"
    "s_add_u32 s73, s77, 40576\n"
    "buffer_load_dwordx4 v21, s[32:35], s73 offen offset:1024 lds\n"
    "v_mfma_f32_16x16x32_bf16 a[216:219], a[232:235], v[32:35], a[216:219]\n"
    "v_mfma_f32_16x16x32_bf16 a[220:223], a[232:235], v[36:39], a[220:223]\n"
    "v_mfma_f32_16x16x32_bf16 a[224:227], a[232:235], v[40:43], a[224:227]\n"
    "s_add_u32 s73, s77, 47744\n"
    "buffer_load_dwordx4 v20, s[32:35], s73 offen offset:2048 lds\n"
    "v_mfma_f32_16x16x32_bf16 a[228:231], a[232:235], v[44:47], a[228:231]\n"
    "v_mfma_f32_16x16x32_bf16 a[236:239], v[32:35], v[32:35], a[236:239]\n"
    "v_mfma_f32_16x16x32_bf16 a[240:243], v[36:39], v[36:39], a[240:243]\n"
    "s_add_u32 s73, s77, 54912\n"
    "buffer_load_dwordx4 v21, s[32:35], s73 offen offset:3072 lds\n"
    "v_mfma_f32_16x16x32_bf16 a[244:247], v[40:43], v[40:43], a[244:247]\n"
    "v_mfma_f32_16x16x32_bf16 a[248:251], v[44:47], v[44:47], a[248:251]\n"
    "s_waitcnt lgkmcnt(0)\n"
    "s_waitcnt vmcnt(12)\n"
    "s_barrier\n"
    "v_add_u32 v22, 16384, v22\n"
    "v_add_u32 v23, 16384, v23\n"
    "s_sub_u32 s75, s83, s82\n"
    "v_add_u32 v24, s75, v24\n"
    "v_mfma_f32_16x16x32_bf16 v[64:67], a[160:163], v[48:51], v[64:67]\n"
    "ds_read_b128 v[32:35], v24 offset:0\n"
    "v_mfma_f32_16x16x32_bf16 v[96:99], a[160:163], v[52:55], v[96:99]\n"
    "ds_read_b128 v[36:39], v24 offset:2048\n"
    "v_mfma_f32_16x16x32_bf16 v[128:131], a[160:163], v[56:59], v[128:131]\n"
    "ds_read_b128 v[40:43], v24 offset:4096\n"
    "v_mfma_f32_16x16x32_bf16 v[160:163], a[160:163], v[60:63], v[160:163]\n"
    "ds_read_b128 v[44:47], v24 offset:6144\n"
    "v_mfma_f32_16x16x32_bf16 v[68:71], a[164:167], v[48:51], v[68:71]\n"
    "ds_read_b64_tr_b16 a[128:129], v22 offset:0\n"
    "v_mfma_f32_16x16x32_bf16 v[100:103], a[164:167], v[52:55], v[100:103]\n"
    "ds_read_b64_tr_b16 a[130:131], v22 offset:256\n"
    "v_mfma_f32_16x16x32_bf16 v[132:135], a[164:167], v[56:59], v[132:135]\n"
    "ds_read_b64_tr_b16 a[132:133], v22 offset:8\n"
    "v_mfma_f32_16x16x32_bf16 v[164:167], a[164:167], v[60:63], v[164:167]\n"
    "ds_read_b64_tr_b16 a[134:135], v22 offset:264\n"
    "v_mfma_f32_16x16x32_bf16 v[72:75], a[168:171], v[48:51], v[72:75]\n"
    "ds_read_b64_tr_b16 a[136:137], v22 offset:512\n"
    "v_mfma_f32_16x16x32_bf16 v[104:107], a[168:171], v[52:55], v[104:107]\n"
    "ds_read_b64_tr_b16 a[138:139], v22 offset:768\n"
    "v_mfma_f32_16x16x32_bf16 v[136:139], a[168:171], v[56:59], v[136:139]\n"
    "ds_read_b64_tr_b16 a[140:141], v22 offset:520\n"
    "v_mfma_f32_16x16x32_bf16 v[168:171], a[168:171], v[60:63], v[168:171]\n"
    "ds_read_b64_tr_b16 a[142:143], v22 offset:776\n"
    "v_mfma_f32_16x16x32_bf16 v[76:79], a[172:175], v[48:51], v[76:79]\n"
    "ds_read_b64_tr_b16 a[144:145], v22 offset:1024\n"
    "v_mfma_f32_16x16x32_bf16 v[108:111], a[172:175], v[52:55], v[108:111]\n"
    "ds_read_b64_tr_b16 a[146:147], v22 offset:1280\n"
    "v_mfma_f32_16x16x32_bf16 v[140:143], a[172:175], v[56:59], v[140:143]\n"
    "ds_read_b64_tr_b16 a[148:149], v22 offset:1032\n"
    "v_mfma_f32_16x16x32_bf16 v[172:175], a[172:175], v[60:63], v[172:175]\n"
    "ds_read_b64_tr_b16 a[150:151], v22 offset:1288\n"
    "v_mfma_f32_16x16x32_bf16 v[80:83], a[176:179], v[48:51], v[80:83]\n"
    "ds_read_b64_tr_b16 a[152:153], v22 offset:1536\n"
    "v_mfma_f32_16x16x32_bf16 v[112:115], a[176:179], v[52:55], v[112:115]\n"
    "ds_read_b64_tr_b16 a[154:155], v22 offset:1792\n"
    "v_mfma_f32_16x16x32_bf16 v[144:147], a[176:179], v[56:59], v[144:147]\n"
    "ds_read_b64_tr_b16 a[156:157], v22 offset:1544\n"
    "v_mfma_f32_16x16x32_bf16 v[176:179], a[176:179], v[60:63], v[176:179]\n"
    "ds_read_b64_tr_b16 a[158:159], v22 offset:1800\n"
    "v_mfma_f32_16x16x32_bf16 v[84:87], a[180:183], v[48:51], v[84:87]\n"
    "ds_read_b64_tr_b16 a[192:193], v23 offset:0\n"
    "v_mfma_f32_16x16x32_bf16 v[116:119], a[180:183], v[52:55], v[116:119]\n"
    "ds_read_b64_tr_b16 a[194:195], v23 offset:256\n"
    "v_mfma_f32_16x16x32_bf16 v[148:151], a[180:183], v[56:59], v[148:151]\n"
    "ds_read_b64_tr_b16 a[196:197], v23 offset:8\n"
    "v_mfma_f32_16x16x32_bf16 v[180:183], a[180:183], v[60:63], v[180:183]\n"
    "ds_read_b64_tr_b16 a[198:199], v23 offset:264\n"
    "v_mfma_f32_16x16x32_bf16 v[88:91], a[184:187], v[48:51], v[88:91]\n"
    "s_add_u32 m0, s70, 32768\n"
    "s_add_u32 s73, s76, 3149824\n"
    "buffer_load_dwordx4 v19, s[36:39], s73 offen offset:0 lds\n"
    "v_mfma_f32_16x16x32_bf16 v[120:123], a[184:187], v[52:55], v[120:123]\n"
    "v_mfma_f32_16x16x32_bf16 v[152:155], a[184:187], v[56:59], v[152:155]\n"
    "v_mfma_f32_16x16x32_bf16 v[184:187], a[184:187], v[60:63], v[184:187]\n"
    "s_add_u32 s73, s76, 3148928\n"
    "buffer_load_dwordx4 v19, s[36:39], s73 offen offset:1024 lds\n"
    "v_mfma_f32_16x16x32_bf16 v[92:95], a[188:191], v[48:51], v[92:95]\n"
    "v_mfma_f32_16x16x32_bf16 v[124:127], a[188:191], v[52:55], v[124:127]\n"
    "v_mfma_f32_16x16x32_bf16 v[156:159], a[188:191], v[56:59], v[156:159]\n"
    "s_add_u32 s73, s76, 3213312\n"
    "buffer_load_dwordx4 v19, s[36:39], s73 offen offset:2048 lds\n"
    "v_mfma_f32_16x16x32_bf16 v[188:191], a[188:191], v[60:63], v[188:191]\n"
    "v_mfma_f32_16x16x32_bf16 a[208:211], a[232:235], a[200:203], a[208:211]\n"
    "v_mfma_f32_16x16x32_bf16 a[212:215], a[232:235], a[204:207], a[212:215]\n"
    "s_add_u32 s73, s76, 3212416\n"
    "buffer_load_dwordx4 v19, s[36:39], s73 offen offset:3072 lds\n"
    "v_mfma_f32_16x16x32_bf16 a[216:219], a[232:235], v[48:51], a[216:219]\n"
    "v_mfma_f32_16x16x32_bf16 a[220:223], a[232:235], v[52:55], a[220:223]\n"
    "v_mfma_f32_16x16x32_bf16 a[224:227], a[232:235], v[56:59], a[224:227]\n"
    "s_add_u32 m0, s82, 0\n"
    "s_add_u32 s73, s77, 768\n"
    "buffer_load_dwordx4 v20, s[32:35], s73 offen offset:0 lds\n"
    "v_mfma_f32_16x16x32_bf16 a[228:231], a[232:235], v[60:63], a[228:231]\n"
    "v_mfma_f32_16x16x32_bf16 a[236:239], v[48:51], v[48:51], a[236:239]\n"
    "v_mfma_f32_16x16x32_bf16 a[240:243], v[52:55], v[52:55], a[240:243]\n"
    "s_add_u32 s73, s77, 7936\n"
    "buffer_load_dwordx4 v21, s[32:35], s73 offen offset:1024 lds\n"
    "v_mfma_f32_16x16x32_bf16 a[244:247], v[56:59], v[56:59], a[244:247]\n"
    "v_mfma_f32_16x16x32_bf16 a[248:251], v[60:63], v[60:63], a[248:251]\n"
    "s_waitcnt lgkmcnt(0)\n"
    "v_mfma_f32_16x16x32_bf16 v[64:67], a[128:131], v[32:35], v[64:67]\n"
    "ds_read_b128 v[48:51], v24 offset:512\n"
    "v_mfma_f32_16x16x32_bf16 v[96:99], a[128:131], v[36:39], v[96:99]\n"
    "ds_read_b128 v[52:55], v24 offset:2560\n"
    "v_mfma_f32_16x16x32_bf16 v[128:131], a[128:131], v[40:43], v[128:131]\n"
    "ds_read_b128 v[56:59], v24 offset:4608\n"
    "v_mfma_f32_16x16x32_bf16 v[160:163], a[128:131], v[44:47], v[160:163]\n"
    "ds_read_b128 v[60:63], v24 offset:6656\n"
    "v_mfma_f32_16x16x32_bf16 v[68:71], a[132:135], v[32:35], v[68:71]\n"
    "ds_read_b64_tr_b16 a[160:161], v22 offset:8192\n"
    "v_mfma_f32_16x16x32_bf16 v[100:103], a[132:135], v[36:39], v[100:103]\n"
    "ds_read_b64_tr_b16 a[162:163], v22 offset:8448\n"
    "v_mfma_f32_16x16x32_bf16 v[132:135], a[132:135], v[40:43], v[132:135]\n"
    "ds_read_b64_tr_b16 a[164:165], v22 offset:8200\n"
    "v_mfma_f32_16x16x32_bf16 v[164:167], a[132:135], v[44:47], v[164:167]\n"
    "ds_read_b64_tr_b16 a[166:167], v22 offset:8456\n"
    "v_mfma_f32_16x16x32_bf16 v[72:75], a[136:139], v[32:35], v[72:75]\n"
    "ds_read_b64_tr_b16 a[168:169], v22 offset:8704\n"
    "v_mfma_f32_16x16x32_bf16 v[104:107], a[136:139], v[36:39], v[104:107]\n"
    "ds_read_b64_tr_b16 a[170:171], v22 offset:8960\n"
    "v_mfma_f32_16x16x32_bf16 v[136:139], a[136:139], v[40:43], v[136:139]\n"
    "ds_read_b64_tr_b16 a[172:173], v22 offset:8712\n"
    "v_mfma_f32_16x16x32_bf16 v[168:171], a[136:139], v[44:47], v[168:171]\n"
    "ds_read_b64_tr_b16 a[174:175], v22 offset:8968\n"
    "v_mfma_f32_16x16x32_bf16 v[76:79], a[140:143], v[32:35], v[76:79]\n"
    "ds_read_b64_tr_b16 a[176:177], v22 offset:9216\n"
    "v_mfma_f32_16x16x32_bf16 v[108:111], a[140:143], v[36:39], v[108:111]\n"
    "ds_read_b64_tr_b16 a[178:179], v22 offset:9472\n"
    "v_mfma_f32_16x16x32_bf16 v[140:143], a[140:143], v[40:43], v[140:143]\n"
    "ds_read_b64_tr_b16 a[180:181], v22 offset:9224\n"
    "v_mfma_f32_16x16x32_bf16 v[172:175], a[140:143], v[44:47], v[172:175]\n"
    "ds_read_b64_tr_b16 a[182:183], v22 offset:9480\n"
    "v_mfma_f32_16x16x32_bf16 v[80:83], a[144:147], v[32:35], v[80:83]\n"
    "ds_read_b64_tr_b16 a[184:185], v22 offset:9728\n"
    "v_mfma_f32_16x16x32_bf16 v[112:115], a[144:147], v[36:39], v[112:115]\n"
    "ds_read_b64_tr_b16 a[186:187], v22 offset:9984\n"
    "v_mfma_f32_16x16x32_bf16 v[144:147], a[144:147], v[40:43], v[144:147]\n"
    "ds_read_b64_tr_b16 a[188:189], v22 offset:9736\n"
    "v_mfma_f32_16x16x32_bf16 v[176:179], a[144:147], v[44:47], v[176:179]\n"
    "ds_read_b64_tr_b16 a[190:191], v22 offset:9992\n"
    "v_mfma_f32_16x16x32_bf16 v[84:87], a[148:151], v[32:35], v[84:87]\n"
    "ds_read_b64_tr_b16 a[200:201], v23 offset:8192\n"
    "v_mfma_f32_16x16x32_bf16 v[116:119], a[148:151], v[36:39], v[116:119]\n"
    "ds_read_b64_tr_b16 a[202:203], v23 offset:8448\n"
    "v_mfma_f32_16x16x32_bf16 v[148:151], a[148:151], v[40:43], v[148:151]\n"
    "ds_read_b64_tr_b16 a[204:205], v23 offset:8200\n"
    "v_mfma_f32_16x16x32_bf16 v[180:183], a[148:151], v[44:47], v[180:183]\n"
    "ds_read_b64_tr_b16 a[206:207], v23 offset:8456\n"
    "v_mfma_f32_16x16x32_bf16 v[88:91], a[152:155], v[32:35], v[88:91]\n"
    "s_add_u32 s73, s77, 15104\n"
    "buffer_load_dwordx4 v20, s[32:35], s73 offen offset:2048 lds\n"
    "v_mfma_f32_16x16x32_bf16 v[120:123], a[152:155], v[36:39], v[120:123]\n"
    "v_mfma_f32_16x16x32_bf16 v[152:155], a[152:155], v[40:43], v[152:155]\n"
    "v_mfma_f32_16x16x32_bf16 v[184:187], a[152:155], v[44:47], v[184:187]\n"
    "s_add_u32 s73, s77, 22272\n"
    "buffer_load_dwordx4 v21, s[32:35], s73 offen offset:3072 lds\n"
    "v_mfma_f32_16x16x32_bf16 v[92:95], a[156:159], v[32:35], v[92:95]\n"
    "v_mfma_f32_16x16x32_bf16 v[124:127], a[156:159], v[36:39], v[124:127]\n"
    "v_mfma_f32_16x16x32_bf16 v[156:159], a[156:159], v[40:43], v[156:159]\n"
    "s_add_u32 m0, s82, 4096\n"
    "s_add_u32 s73, s77, 33536\n"
    "buffer_load_dwordx4 v20, s[32:35], s73 offen offset:0 lds\n"
    "v_mfma_f32_16x16x32_bf16 v[188:191], a[156:159], v[44:47], v[188:191]\n"
    "v_mfma_f32_16x16x32_bf16 a[208:211], a[232:235], a[192:195], a[208:211]\n"
    "v_mfma_f32_16x16x32_bf16 a[212:215], a[232:235], a[196:199], a[212:215]\n"
    "s_add_u32 s73, s77, 40704\n"
    "buffer_load_dwordx4 v21, s[32:35], s73 offen offset:1024 lds\n"
    "v_mfma_f32_16x16x32_bf16 a[216:219], a[232:235], v[32:35], a[216:219]\n"
    "v_mfma_f32_16x16x32_bf16 a[220:223], a[232:235], v[36:39], a[220:223]\n"
    "v_mfma_f32_16x16x32_bf16 a[224:227], a[232:235], v[40:43], a[224:227]\n"
    "s_add_u32 s73, s77, 47872\n"
    "buffer_load_dwordx4 v20, s[32:35], s73 offen offset:2048 lds\n"
    "v_mfma_f32_16x16x32_bf16 a[228:231], a[232:235], v[44:47], a[228:231]\n"
    "v_mfma_f32_16x16x32_bf16 a[236:239], v[32:35], v[32:35], a[236:239]\n"
    "v_mfma_f32_16x16x32_bf16 a[240:243], v[36:39], v[36:39], a[240:243]\n"
    "s_add_u32 s73, s77, 55040\n"
    "buffer_load_dwordx4 v21, s[32:35], s73 offen offset:3072 lds\n"
    "v_mfma_f32_16x16x32_bf16 a[244:247], v[40:43], v[40:43], a[244:247]\n"
    "v_mfma_f32_16x16x32_bf16 a[248:251], v[44:47], v[44:47], a[248:251]\n"
    "s_waitcnt lgkmcnt(0)\n"
    "s_waitcnt vmcnt(12)\n"
    "s_barrier\n"
    "v_add_u32 v22, 81920, v22\n"
    "v_add_u32 v23, 81920, v23\n"
    "s_sub_u32 s75, s78, s83\n"
    "v_add_u32 v24, s75, v24\n"
    "v_mfma_f32_16x16x32_bf16 v[64:67], a[160:163], v[48:51], v[64:67]\n"
    "ds_read_b128 v[32:35], v24 offset:0\n"
    "v_mfma_f32_16x16x32_bf16 v[96:99], a[160:163], v[52:55], v[96:99]\n"
    "ds_read_b128 v[36:39], v24 offset:2048\n"
    "v_mfma_f32_16x16x32_bf16 v[128:131], a[160:163], v[56:59], v[128:131]\n"
    "ds_read_b128 v[40:43], v24 offset:4096\n"
    "v_mfma_f32_16x16x32_bf16 v[160:163], a[160:163], v[60:63], v[160:163]\n"
    "ds_read_b128 v[44:47], v24 offset:6144\n"
    "v_mfma_f32_16x16x32_bf16 v[68:71], a[164:167], v[48:51], v[68:71]\n"
    "ds_read_b64_tr_b16 a[128:129], v22 offset:0\n"
    "v_mfma_f32_16x16x32_bf16 v[100:103], a[164:167], v[52:55], v[100:103]\n"
    "ds_read_b64_tr_b16 a[130:131], v22 offset:256\n"
    "v_mfma_f32_16x16x32_bf16 v[132:135], a[164:167], v[56:59], v[132:135]\n"
    "ds_read_b64_tr_b16 a[132:133], v22 offset:8\n"
    "v_mfma_f32_16x16x32_bf16 v[164:167], a[164:167], v[60:63], v[164:167]\n"
    "ds_read_b64_tr_b16 a[134:135], v22 offset:264\n"
    "v_mfma_f32_16x16x32_bf16 v[72:75], a[168:171], v[48:51], v[72:75]\n"
    "ds_read_b64_tr_b16 a[136:137], v22 offset:512\n"
    "v_mfma_f32_16x16x32_bf16 v[104:107], a[168:171], v[52:55], v[104:107]\n"
    "ds_read_b64_tr_b16 a[138:139], v22 offset:768\n"
    "v_mfma_f32_16x16x32_bf16 v[136:139], a[168:171], v[56:59], v[136:139]\n"
    "ds_read_b64_tr_b16 a[140:141], v22 offset:520\n"
    "v_mfma_f32_16x16x32_bf16 v[168:171], a[168:171], v[60:63], v[168:171]\n"
    "ds_read_b64_tr_b16 a[142:143], v22 offset:776\n"
    "v_mfma_f32_16x16x32_bf16 v[76:79], a[172:175], v[48:51], v[76:79]\n"
    "ds_read_b64_tr_b16 a[144:145], v22 offset:1024\n"
    "v_mfma_f32_16x16x32_bf16 v[108:111], a[172:175], v[52:55], v[108:111]\n"
    "ds_read_b64_tr_b16 a[146:147], v22 offset:1280\n"
    "v_mfma_f32_16x16x32_bf16 v[140:143], a[172:175], v[56:59], v[140:143]\n"
    "ds_read_b64_tr_b16 a[148:149], v22 offset:1032\n"
    "v_mfma_f32_16x16x32_bf16 v[172:175], a[172:175], v[60:63], v[172:175]\n"
    "ds_read_b64_tr_b16 a[150:151], v22 offset:1288\n"
    "v_mfma_f32_16x16x32_bf16 v[80:83], a[176:179], v[48:51], v[80:83]\n"
    "ds_read_b64_tr_b16 a[152:153], v22 offset:1536\n"
    "v_mfma_f32_16x16x32_bf16 v[112:115], a[176:179], v[52:55], v[112:115]\n"
    "ds_read_b64_tr_b16 a[154:155], v22 offset:1792\n"
    "v_mfma_f32_16x16x32_bf16 v[144:147], a[176:179], v[56:59], v[144:147]\n"
    "ds_read_b64_tr_b16 a[156:157], v22 offset:1544\n"
    "v_mfma_f32_16x16x32_bf16 v[176:179], a[176:179], v[60:63], v[176:179]\n"
    "ds_read_b64_tr_b16 a[158:159], v22 offset:1800\n"
    "v_mfma_f32_16x16x32_bf16 v[84:87], a[180:183], v[48:51], v[84:87]\n"
    "ds_read_b64_tr_b16 a[192:193], v23 offset:0\n"
    "v_mfma_f32_16x16x32_bf16 v[116:119], a[180:183], v[52:55], v[116:119]\n"
    "ds_read_b64_tr_b16 a[194:195], v23 offset:256\n"
    "v_mfma_f32_16x16x32_bf16 v[148:151], a[180:183], v[56:59], v[148:151]\n"
    "ds_read_b64_tr_b16 a[196:197], v23 offset:8\n"
    "v_mfma_f32_16x16x32_bf16 v[180:183], a[180:183], v[60:63], v[180:183]\n"
    "ds_read_b64_tr_b16 a[198:199], v23 offset:264\n"
    "v_mfma_f32_16x16x32_bf16 v[88:91], a[184:187], v[48:51], v[88:91]\n"
    "s_add_u32 m0, s70, 49152\n"
    "s_add_u32 s73, s76, 3674112\n"
    "buffer_load_dwordx4 v19, s[36:39], s73 offen offset:0 lds\n"
    "v_mfma_f32_16x16x32_bf16 v[120:123], a[184:187], v[52:55], v[120:123]\n"
    "v_mfma_f32_16x16x32_bf16 v[152:155], a[184:187], v[56:59], v[152:155]\n"
    "v_mfma_f32_16x16x32_bf16 v[184:187], a[184:187], v[60:63], v[184:187]\n"
    "s_add_u32 s73, s76, 3673216\n"
    "buffer_load_dwordx4 v19, s[36:39], s73 offen offset:1024 lds\n"
    "v_mfma_f32_16x16x32_bf16 v[92:95], a[188:191], v[48:51], v[92:95]\n"
    "v_mfma_f32_16x16x32_bf16 v[124:127], a[188:191], v[52:55], v[124:127]\n"
    "v_mfma_f32_16x16x32_bf16 v[156:159], a[188:191], v[56:59], v[156:159]\n"
    "s_add_u32 s73, s76, 3737600\n"
    "buffer_load_dwordx4 v19, s[36:39], s73 offen offset:2048 lds\n"
    "v_mfma_f32_16x16x32_bf16 v[188:191], a[188:191], v[60:63], v[188:191]\n"
    "v_mfma_f32_16x16x32_bf16 a[208:211], a[232:235], a[200:203], a[208:211]\n"
    "v_mfma_f32_16x16x32_bf16 a[212:215], a[232:235], a[204:207], a[212:215]\n"
    "s_add_u32 s73, s76, 3736704\n"
    "buffer_load_dwordx4 v19, s[36:39], s73 offen offset:3072 lds\n"
    "v_mfma_f32_16x16x32_bf16 a[216:219], a[232:235], v[48:51], a[216:219]\n"
    "v_mfma_f32_16x16x32_bf16 a[220:223], a[232:235], v[52:55], a[220:223]\n"
    "v_mfma_f32_16x16x32_bf16 a[224:227], a[232:235], v[56:59], a[224:227]\n"
    "s_add_u32 m0, s83, 0\n"
    "s_add_u32 s73, s77, 896\n"
    "buffer_load_dwordx4 v20, s[32:35], s73 offen offset:0 lds\n"
    "v_mfma_f32_16x16x32_bf16 a[228:231], a[232:235], v[60:63], a[228:231]\n"
    "v_mfma_f32_16x16x32_bf16 a[236:239], v[48:51], v[48:51], a[236:239]\n"
    "v_mfma_f32_16x16x32_bf16 a[240:243], v[52:55], v[52:55], a[240:243]\n"
    "s_add_u32 s73, s77, 8064\n"
    "buffer_load_dwordx4 v21, s[32:35], s73 offen offset:1024 lds\n"
    "v_mfma_f32_16x16x32_bf16 a[244:247], v[56:59], v[56:59], a[244:247]\n"
    "v_mfma_f32_16x16x32_bf16 a[248:251], v[60:63], v[60:63], a[248:251]\n"
    "s_waitcnt lgkmcnt(0)\n"
    "v_mfma_f32_16x16x32_bf16 v[64:67], a[128:131], v[32:35], v[64:67]\n"
    "ds_read_b128 v[48:51], v24 offset:512\n"
    "v_mfma_f32_16x16x32_bf16 v[96:99], a[128:131], v[36:39], v[96:99]\n"
    "ds_read_b128 v[52:55], v24 offset:2560\n"
    "v_mfma_f32_16x16x32_bf16 v[128:131], a[128:131], v[40:43], v[128:131]\n"
    "ds_read_b128 v[56:59], v24 offset:4608\n"
    "v_mfma_f32_16x16x32_bf16 v[160:163], a[128:131], v[44:47], v[160:163]\n"
    "ds_read_b128 v[60:63], v24 offset:6656\n"
    "v_mfma_f32_16x16x32_bf16 v[68:71], a[132:135], v[32:35], v[68:71]\n"
    "ds_read_b64_tr_b16 a[160:161], v22 offset:8192\n"
    "v_mfma_f32_16x16x32_bf16 v[100:103], a[132:135], v[36:39], v[100:103]\n"
    "ds_read_b64_tr_b16 a[162:163], v22 offset:8448\n"
    "v_mfma_f32_16x16x32_bf16 v[132:135], a[132:135], v[40:43], v[132:135]\n"
    "ds_read_b64_tr_b16 a[164:165], v22 offset:8200\n"
    "v_mfma_f32_16x16x32_bf16 v[164:167], a[132:135], v[44:47], v[164:167]\n"
    "ds_read_b64_tr_b16 a[166:167], v22 offset:8456\n"
    "v_mfma_f32_16x16x32_bf16 v[72:75], a[136:139], v[32:35], v[72:75]\n"
    "ds_read_b64_tr_b16 a[168:169], v22 offset:8704\n"
    "v_mfma_f32_16x16x32_bf16 v[104:107], a[136:139], v[36:39], v[104:107]\n"
    "ds_read_b64_tr_b16 a[170:171], v22 offset:8960\n"
    "v_mfma_f32_16x16x32_bf16 v[136:139], a[136:139], v[40:43], v[136:139]\n"
    "ds_read_b64_tr_b16 a[172:173], v22 offset:8712\n"
    "v_mfma_f32_16x16x32_bf16 v[168:171], a[136:139], v[44:47], v[168:171]\n"
    "ds_read_b64_tr_b16 a[174:175], v22 offset:8968\n"
    "v_mfma_f32_16x16x32_bf16 v[76:79], a[140:143], v[32:35], v[76:79]\n"
    "ds_read_b64_tr_b16 a[176:177], v22 offset:9216\n"
    "v_mfma_f32_16x16x32_bf16 v[108:111], a[140:143], v[36:39], v[108:111]\n"
    "ds_read_b64_tr_b16 a[178:179], v22 offset:9472\n"
    "v_mfma_f32_16x16x32_bf16 v[140:143], a[140:143], v[40:43], v[140:143]\n"
    "ds_read_b64_tr_b16 a[180:181], v22 offset:9224\n"
    "v_mfma_f32_16x16x32_bf16 v[172:175], a[140:143], v[44:47], v[172:175]\n"
    "ds_read_b64_tr_b16 a[182:183], v22 offset:9480\n"
    "v_mfma_f32_16x16x32_bf16 v[80:83], a[144:147], v[32:35], v[80:83]\n"
    "ds_read_b64_tr_b16 a[184:185], v22 offset:9728\n"
    "v_mfma_f32_16x16x32_bf16 v[112:115], a[144:147], v[36:39], v[112:115]\n"
    "ds_read_b64_tr_b16 a[186:187], v22 offset:9984\n"
    "v_mfma_f32_16x16x32_bf16 v[144:147], a[144:147], v[40:43], v[144:147]\n"
    "ds_read_b64_tr_b16 a[188:189], v22 offset:9736\n"
    "v_mfma_f32_16x16x32_bf16 v[176:179], a[144:147], v[44:47], v[176:179]\n"
    "ds_read_b64_tr_b16 a[190:191], v22 offset:9992\n"
    "v_mfma_f32_16x16x32_bf16 v[84:87], a[148:151], v[32:35], v[84:87]\n"
    "ds_read_b64_tr_b16 a[200:201], v23 offset:8192\n"
    "v_mfma_f32_16x16x32_bf16 v[116:119], a[148:151], v[36:39], v[116:119]\n"
    "ds_read_b64_tr_b16 a[202:203], v23 offset:8448\n"
    "v_mfma_f32_16x16x32_bf16 v[148:151], a[148:151], v[40:43], v[148:151]\n"
    "ds_read_b64_tr_b16 a[204:205], v23 offset:8200\n"
    "v_mfma_f32_16x16x32_bf16 v[180:183], a[148:151], v[44:47], v[180:183]\n"
    "ds_read_b64_tr_b16 a[206:207], v23 offset:8456\n"
    "v_mfma_f32_16x16x32_bf16 v[88:91], a[152:155], v[32:35], v[88:91]\n"
    "s_add_u32 s73, s77, 15232\n"
    "buffer_load_dwordx4 v20, s[32:35], s73 offen offset:2048 lds\n"
    "v_mfma_f32_16x16x32_bf16 v[120:123], a[152:155], v[36:39], v[120:123]\n"
    "v_mfma_f32_16x16x32_bf16 v[152:155], a[152:155], v[40:43], v[152:155]\n"
    "v_mfma_f32_16x16x32_bf16 v[184:187], a[152:155], v[44:47], v[184:187]\n"
    "s_add_u32 s73, s77, 22400\n"
    "buffer_load_dwordx4 v21, s[32:35], s73 offen offset:3072 lds\n"
    "v_mfma_f32_16x16x32_bf16 v[92:95], a[156:159], v[32:35], v[92:95]\n"
    "v_mfma_f32_16x16x32_bf16 v[124:127], a[156:159], v[36:39], v[124:127]\n"
    "v_mfma_f32_16x16x32_bf16 v[156:159], a[156:159], v[40:43], v[156:159]\n"
    "s_add_u32 m0, s83, 4096\n"
    "s_add_u32 s73, s77, 33664\n"
    "buffer_load_dwordx4 v20, s[32:35], s73 offen offset:0 lds\n"
    "v_mfma_f32_16x16x32_bf16 v[188:191], a[156:159], v[44:47], v[188:191]\n"
    "v_mfma_f32_16x16x32_bf16 a[208:211], a[232:235], a[192:195], a[208:211]\n"
    "v_mfma_f32_16x16x32_bf16 a[212:215], a[232:235], a[196:199], a[212:215]\n"
    "s_add_u32 s73, s77, 40832\n"
    "buffer_load_dwordx4 v21, s[32:35], s73 offen offset:1024 lds\n"
    "v_mfma_f32_16x16x32_bf16 a[216:219], a[232:235], v[32:35], a[216:219]\n"
    "v_mfma_f32_16x16x32_bf16 a[220:223], a[232:235], v[36:39], a[220:223]\n"
    "v_mfma_f32_16x16x32_bf16 a[224:227], a[232:235], v[40:43], a[224:227]\n"
    "s_add_u32 s73, s77, 48000\n"
    "buffer_load_dwordx4 v20, s[32:35], s73 offen offset:2048 lds\n"
    "v_mfma_f32_16x16x32_bf16 a[228:231], a[232:235], v[44:47], a[228:231]\n"
    "v_mfma_f32_16x16x32_bf16 a[236:239], v[32:35], v[32:35], a[236:239]\n"
    "v_mfma_f32_16x16x32_bf16 a[240:243], v[36:39], v[36:39], a[240:243]\n"
    "s_add_u32 s73, s77, 55168\n"
    "buffer_load_dwordx4 v21, s[32:35], s73 offen offset:3072 lds\n"
    "v_mfma_f32_16x16x32_bf16 a[244:247], v[40:43], v[40:43], a[244:247]\n"
    "v_mfma_f32_16x16x32_bf16 a[248:251], v[44:47], v[44:47], a[248:251]\n"
    "s_waitcnt lgkmcnt(0)\n"
    "s_waitcnt vmcnt(12)\n"
    "s_barrier\n"
    "v_add_u32 v22, 4294868992, v22\n"
    "v_add_u32 v23, 4294868992, v23\n"
    "s_sub_u32 s75, s82, s78\n"
    "v_add_u32 v24, s75, v24\n"
    "v_mfma_f32_16x16x32_bf16 v[64:67], a[160:163], v[48:51], v[64:67]\n"
    "ds_read_b128 v[32:35], v24 offset:0\n"
    "v_mfma_f32_16x16x32_bf16 v[96:99], a[160:163], v[52:55], v[96:99]\n"
    "ds_read_b128 v[36:39], v24 offset:2048\n"
    "v_mfma_f32_16x16x32_bf16 v[128:131], a[160:163], v[56:59], v[128:131]\n"
    "ds_read_b128 v[40:43], v24 offset:4096\n"
    "v_mfma_f32_16x16x32_bf16 v[160:163], a[160:163], v[60:63], v[160:163]\n"
    "ds_read_b128 v[44:47], v24 offset:6144\n"
    "v_mfma_f32_16x16x32_bf16 v[68:71], a[164:167], v[48:51], v[68:71]\n"
    "ds_read_b64_tr_b16 a[128:129], v22 offset:0\n"
    "v_mfma_f32_16x16x32_bf16 v[100:103], a[164:167], v[52:55], v[100:103]\n"
    "ds_read_b64_tr_b16 a[130:131], v22 offset:256\n"
    "v_mfma_f32_16x16x32_bf16 v[132:135], a[164:167], v[56:59], v[132:135]\n"
    "ds_read_b64_tr_b16 a[132:133], v22 offset:8\n"
    "v_mfma_f32_16x16x32_bf16 v[164:167], a[164:167], v[60:63], v[164:167]\n"
    "ds_read_b64_tr_b16 a[134:135], v22 offset:264\n"
    "v_mfma_f32_16x16x32_bf16 v[72:75], a[168:171], v[48:51], v[72:75]\n"
    "ds_read_b64_tr_b16 a[136:137], v22 offset:512\n"
    "v_mfma_f32_16x16x32_bf16 v[104:107], a[168:171], v[52:55], v[104:107]\n"
    "ds_read_b64_tr_b16 a[138:139], v22 offset:768\n"
    "v_mfma_f32_16x16x32_bf16 v[136:139], a[168:171], v[56:59], v[136:139]\n"
    "ds_read_b64_tr_b16 a[140:141], v22 offset:520\n"
    "v_mfma_f32_16x16x32_bf16 v[168:171], a[168:171], v[60:63], v[168:171]\n"
    "ds_read_b64_tr_b16 a[142:143], v22 offset:776\n"
    "v_mfma_f32_16x16x32_bf16 v[76:79], a[172:175], v[48:51], v[76:79]\n"
    "ds_read_b64_tr_b16 a[144:145], v22 offset:1024\n"
    "v_mfma_f32_16x16x32_bf16 v[108:111], a[172:175], v[52:55], v[108:111]\n"
    "ds_read_b64_tr_b16 a[146:147], v22 offset:1280\n"
    "v_mfma_f32_16x16x32_bf16 v[140:143], a[172:175], v[56:59], v[140:143]\n"
    "ds_read_b64_tr_b16 a[148:149], v22 offset:1032\n"
    "v_mfma_f32_16x16x32_bf16 v[172:175], a[172:175], v[60:63], v[172:175]\n"
    "ds_read_b64_tr_b16 a[150:151], v22 offset:1288\n"
    "v_mfma_f32_16x16x32_bf16 v[80:83], a[176:179], v[48:51], v[80:83]\n"
    "ds_read_b64_tr_b16 a[152:153], v22 offset:1536\n"
    "v_mfma_f32_16x16x32_bf16 v[112:115], a[176:179], v[52:55], v[112:115]\n"
    "ds_read_b64_tr_b16 a[154:155], v22 offset:1792\n"
    "v_mfma_f32_16x16x32_bf16 v[144:147], a[176:179], v[56:59], v[144:147]\n"
    "ds_read_b64_tr_b16 a[156:157], v22 offset:1544\n"
    "v_mfma_f32_16x16x32_bf16 v[176:179], a[176:179], v[60:63], v[176:179]\n"
    "ds_read_b64_tr_b16 a[158:159], v22 offset:1800\n"
    "v_mfma_f32_16x16x32_bf16 v[84:87], a[180:183], v[48:51], v[84:87]\n"
    "ds_read_b64_tr_b16 a[192:193], v23 offset:0\n"
    "v_mfma_f32_16x16x32_bf16 v[116:119], a[180:183], v[52:55], v[116:119]\n"
    "ds_read_b64_tr_b16 a[194:195], v23 offset:256\n"
    "v_mfma_f32_16x16x32_bf16 v[148:151], a[180:183], v[56:59], v[148:151]\n"
    "ds_read_b64_tr_b16 a[196:197], v23 offset:8\n"
    "v_mfma_f32_16x16x32_bf16 v[180:183], a[180:183], v[60:63], v[180:183]\n"
    "ds_read_b64_tr_b16 a[198:199], v23 offset:264\n"
    "v_mfma_f32_16x16x32_bf16 v[88:91], a[184:187], v[48:51], v[88:91]\n"
    "s_add_u32 m0, s70, 16384\n"
    "s_add_u32 s74, s80, 16384\n"
    "s_and_b32 s74, s74, 0x7ffff\n"
    "buffer_load_dwordx4 v5, s[40:43], s74 offen offset:0 lds\n"
    "v_mfma_f32_16x16x32_bf16 v[120:123], a[184:187], v[52:55], v[120:123]\n"
    "v_mfma_f32_16x16x32_bf16 v[152:155], a[184:187], v[56:59], v[152:155]\n"
    "v_mfma_f32_16x16x32_bf16 v[184:187], a[184:187], v[60:63], v[184:187]\n"
    "buffer_load_dwordx4 v6, s[40:43], s74 offen offset:1024 lds\n"
    "v_mfma_f32_16x16x32_bf16 v[92:95], a[188:191], v[48:51], v[92:95]\n"
    "v_mfma_f32_16x16x32_bf16 v[124:127], a[188:191], v[52:55], v[124:127]\n"
    "v_mfma_f32_16x16x32_bf16 v[156:159], a[188:191], v[56:59], v[156:159]\n"
    "buffer_load_dwordx4 v7, s[40:43], s74 offen offset:2048 lds\n"
    "v_mfma_f32_16x16x32_bf16 v[188:191], a[188:191], v[60:63], v[188:191]\n"
    "v_mfma_f32_16x16x32_bf16 a[208:211], a[232:235], a[200:203], a[208:211]\n"
    "v_mfma_f32_16x16x32_bf16 a[212:215], a[232:235], a[204:207], a[212:215]\n"
    "buffer_load_dwordx4 v8, s[40:43], s74 offen offset:3072 lds\n"
    "v_mfma_f32_16x16x32_bf16 a[216:219], a[232:235], v[48:51], a[216:219]\n"
    "v_mfma_f32_16x16x32_bf16 a[220:223], a[232:235], v[52:55], a[220:223]\n"
    "v_mfma_f32_16x16x32_bf16 a[224:227], a[232:235], v[56:59], a[224:227]\n"
    "s_add_u32 m0, s71, 0\n"
    "s_add_u32 s74, s80, 0\n"
    "s_and_b32 s74, s74, 0x7ffff\n"
    "buffer_load_dwordx4 v9, s[44:47], s74 offen offset:0 lds\n"
    "v_mfma_f32_16x16x32_bf16 a[228:231], a[232:235], v[60:63], a[228:231]\n"
    "v_mfma_f32_16x16x32_bf16 a[236:239], v[48:51], v[48:51], a[236:239]\n"
    "v_mfma_f32_16x16x32_bf16 a[240:243], v[52:55], v[52:55], a[240:243]\n"
    "buffer_load_dwordx4 v10, s[44:47], s74 offen offset:1024 lds\n"
    "v_mfma_f32_16x16x32_bf16 a[244:247], v[56:59], v[56:59], a[244:247]\n"
    "v_mfma_f32_16x16x32_bf16 a[248:251], v[60:63], v[60:63], a[248:251]\n"
    "s_waitcnt lgkmcnt(0)\n"
    "v_mfma_f32_16x16x32_bf16 v[64:67], a[128:131], v[32:35], v[64:67]\n"
    "ds_read_b128 v[48:51], v24 offset:512\n"
    "v_mfma_f32_16x16x32_bf16 v[96:99], a[128:131], v[36:39], v[96:99]\n"
    "ds_read_b128 v[52:55], v24 offset:2560\n"
    "v_mfma_f32_16x16x32_bf16 v[128:131], a[128:131], v[40:43], v[128:131]\n"
    "ds_read_b128 v[56:59], v24 offset:4608\n"
    "v_mfma_f32_16x16x32_bf16 v[160:163], a[128:131], v[44:47], v[160:163]\n"
    "ds_read_b128 v[60:63], v24 offset:6656\n"
    "v_mfma_f32_16x16x32_bf16 v[68:71], a[132:135], v[32:35], v[68:71]\n"
    "ds_read_b64_tr_b16 a[160:161], v22 offset:8192\n"
    "v_mfma_f32_16x16x32_bf16 v[100:103], a[132:135], v[36:39], v[100:103]\n"
    "ds_read_b64_tr_b16 a[162:163], v22 offset:8448\n"
    "v_mfma_f32_16x16x32_bf16 v[132:135], a[132:135], v[40:43], v[132:135]\n"
    "ds_read_b64_tr_b16 a[164:165], v22 offset:8200\n"
    "v_mfma_f32_16x16x32_bf16 v[164:167], a[132:135], v[44:47], v[164:167]\n"
    "ds_read_b64_tr_b16 a[166:167], v22 offset:8456\n"
    "v_mfma_f32_16x16x32_bf16 v[72:75], a[136:139], v[32:35], v[72:75]\n"
    "ds_read_b64_tr_b16 a[168:169], v22 offset:8704\n"
    "v_mfma_f32_16x16x32_bf16 v[104:107], a[136:139], v[36:39], v[104:107]\n"
    "ds_read_b64_tr_b16 a[170:171], v22 offset:8960\n"
    "v_mfma_f32_16x16x32_bf16 v[136:139], a[136:139], v[40:43], v[136:139]\n"
    "ds_read_b64_tr_b16 a[172:173], v22 offset:8712\n"
    "v_mfma_f32_16x16x32_bf16 v[168:171], a[136:139], v[44:47], v[168:171]\n"
    "ds_read_b64_tr_b16 a[174:175], v22 offset:8968\n"
    "v_mfma_f32_16x16x32_bf16 v[76:79], a[140:143], v[32:35], v[76:79]\n"
    "ds_read_b64_tr_b16 a[176:177], v22 offset:9216\n"
    "v_mfma_f32_16x16x32_bf16 v[108:111], a[140:143], v[36:39], v[108:111]\n"
    "ds_read_b64_tr_b16 a[178:179], v22 offset:9472\n"
    "v_mfma_f32_16x16x32_bf16 v[140:143], a[140:143], v[40:43], v[140:143]\n"
    "ds_read_b64_tr_b16 a[180:181], v22 offset:9224\n"
    "v_mfma_f32_16x16x32_bf16 v[172:175], a[140:143], v[44:47], v[172:175]\n"
    "ds_read_b64_tr_b16 a[182:183], v22 offset:9480\n"
    "v_mfma_f32_16x16x32_bf16 v[80:83], a[144:147], v[32:35], v[80:83]\n"
    "ds_read_b64_tr_b16 a[184:185], v22 offset:9728\n"
    "v_mfma_f32_16x16x32_bf16 v[112:115], a[144:147], v[36:39], v[112:115]\n"
    "ds_read_b64_tr_b16 a[186:187], v22 offset:9984\n"
    "v_mfma_f32_16x16x32_bf16 v[144:147], a[144:147], v[40:43], v[144:147]\n"
    "ds_read_b64_tr_b16 a[188:189], v22 offset:9736\n"
    "v_mfma_f32_16x16x32_bf16 v[176:179], a[144:147], v[44:47], v[176:179]\n"
    "ds_read_b64_tr_b16 a[190:191], v22 offset:9992\n"
    "v_mfma_f32_16x16x32_bf16 v[84:87], a[148:151], v[32:35], v[84:87]\n"
    "ds_read_b64_tr_b16 a[200:201], v23 offset:8192\n"
    "v_mfma_f32_16x16x32_bf16 v[116:119], a[148:151], v[36:39], v[116:119]\n"
    "ds_read_b64_tr_b16 a[202:203], v23 offset:8448\n"
    "v_mfma_f32_16x16x32_bf16 v[148:151], a[148:151], v[40:43], v[148:151]\n"
    "ds_read_b64_tr_b16 a[204:205], v23 offset:8200\n"
    "v_mfma_f32_16x16x32_bf16 v[180:183], a[148:151], v[44:47], v[180:183]\n"
    "ds_read_b64_tr_b16 a[206:207], v23 offset:8456\n"
    "v_mfma_f32_16x16x32_bf16 v[88:91], a[152:155], v[32:35], v[88:91]\n"
    "buffer_load_dwordx4 v9, s[44:47], s74 offen offset:2048 lds\n"
    "v_mfma_f32_16x16x32_bf16 v[120:123], a[152:155], v[36:39], v[120:123]\n"
    "v_mfma_f32_16x16x32_bf16 v[152:155], a[152:155], v[40:43], v[152:155]\n"
    "v_mfma_f32_16x16x32_bf16 v[184:187], a[152:155], v[44:47], v[184:187]\n"
    "buffer_load_dwordx4 v10, s[44:47], s74 offen offset:3072 lds\n"
    "v_mfma_f32_16x16x32_bf16 v[92:95], a[156:159], v[32:35], v[92:95]\n"
    "v_mfma_f32_16x16x32_bf16 v[124:127], a[156:159], v[36:39], v[124:127]\n"
    "v_mfma_f32_16x16x32_bf16 v[156:159], a[156:159], v[40:43], v[156:159]\n"
    "v_mfma_f32_16x16x32_bf16 v[188:191], a[156:159], v[44:47], v[188:191]\n"
    "v_mfma_f32_16x16x32_bf16 a[208:211], a[232:235], a[192:195], a[208:211]\n"
    "v_mfma_f32_16x16x32_bf16 a[212:215], a[232:235], a[196:199], a[212:215]\n"
    "v_mfma_f32_16x16x32_bf16 a[216:219], a[232:235], v[32:35], a[216:219]\n"
    "v_mfma_f32_16x16x32_bf16 a[220:223], a[232:235], v[36:39], a[220:223]\n"
    "v_mfma_f32_16x16x32_bf16 a[224:227], a[232:235], v[40:43], a[224:227]\n"
    "v_mfma_f32_16x16x32_bf16 a[228:231], a[232:235], v[44:47], a[228:231]\n"
    "v_mfma_f32_16x16x32_bf16 a[236:239], v[32:35], v[32:35], a[236:239]\n"
    "v_mfma_f32_16x16x32_bf16 a[240:243], v[36:39], v[36:39], a[240:243]\n"
    "v_mfma_f32_16x16x32_bf16 a[244:247], v[40:43], v[40:43], a[244:247]\n"
    "v_mfma_f32_16x16x32_bf16 a[248:251], v[44:47], v[44:47], a[248:251]\n"
    "s_waitcnt lgkmcnt(0)\n"
    "s_waitcnt vmcnt(8)\n"
    "s_barrier\n"
    "v_add_u32 v22, 16384, v22\n"
    "v_add_u32 v23, 16384, v23\n"
    "s_sub_u32 s75, s83, s82\n"
    "v_add_u32 v24, s75, v24\n"
    "v_mfma_f32_16x16x32_bf16 v[64:67], a[160:163], v[48:51], v[64:67]\n"
    "ds_read_b128 v[32:35], v24 offset:0\n"
    "v_mfma_f32_16x16x32_bf16 v[96:99], a[160:163], v[52:55], v[96:99]\n"
    "ds_read_b128 v[36:39], v24 offset:2048\n"
    "v_mfma_f32_16x16x32_bf16 v[128:131], a[160:163], v[56:59], v[128:131]\n"
    "ds_read_b128 v[40:43], v24 offset:4096\n"
    "v_mfma_f32_16x16x32_bf16 v[160:163], a[160:163], v[60:63], v[160:163]\n"
    "ds_read_b128 v[44:47], v24 offset:6144\n"
    "v_mfma_f32_16x16x32_bf16 v[68:71], a[164:167], v[48:51], v[68:71]\n"
    "ds_read_b64_tr_b16 a[128:129], v22 offset:0\n"
    "v_mfma_f32_16x16x32_bf16 v[100:103], a[164:167], v[52:55], v[100:103]\n"
    "ds_read_b64_tr_b16 a[130:131], v22 offset:256\n"
    "v_mfma_f32_16x16x32_bf16 v[132:135], a[164:167], v[56:59], v[132:135]\n"
    "ds_read_b64_tr_b16 a[132:133], v22 offset:8\n"
    "v_mfma_f32_16x16x32_bf16 v[164:167], a[164:167], v[60:63], v[164:167]\n"
    "ds_read_b64_tr_b16 a[134:135], v22 offset:264\n"
    "v_mfma_f32_16x16x32_bf16 v[72:75], a[168:171], v[48:51], v[72:75]\n"
    "ds_read_b64_tr_b16 a[136:137], v22 offset:512\n"
    "v_mfma_f32_16x16x32_bf16 v[104:107], a[168:171], v[52:55], v[104:107]\n"
    "ds_read_b64_tr_b16 a[138:139], v22 offset:768\n"
    "v_mfma_f32_16x16x32_bf16 v[136:139], a[168:171], v[56:59], v[136:139]\n"
    "ds_read_b64_tr_b16 a[140:141], v22 offset:520\n"
    "v_mfma_f32_16x16x32_bf16 v[168:171], a[168:171], v[60:63], v[168:171]\n"
    "ds_read_b64_tr_b16 a[142:143], v22 offset:776\n"
    "v_mfma_f32_16x16x32_bf16 v[76:79], a[172:175], v[48:51], v[76:79]\n"
    "ds_read_b64_tr_b16 a[144:145], v22 offset:1024\n"
    "v_mfma_f32_16x16x32_bf16 v[108:111], a[172:175], v[52:55], v[108:111]\n"
    "ds_read_b64_tr_b16 a[146:147], v22 offset:1280\n"
    "v_mfma_f32_16x16x32_bf16 v[140:143], a[172:175], v[56:59], v[140:143]\n"
    "ds_read_b64_tr_b16 a[148:149], v22 offset:1032\n"
    "v_mfma_f32_16x16x32_bf16 v[172:175], a[172:175], v[60:63], v[172:175]\n"
    "ds_read_b64_tr_b16 a[150:151], v22 offset:1288\n"
    "v_mfma_f32_16x16x32_bf16 v[80:83], a[176:179], v[48:51], v[80:83]\n"
    "ds_read_b64_tr_b16 a[152:153], v22 offset:1536\n"
    "v_mfma_f32_16x16x32_bf16 v[112:115], a[176:179], v[52:55], v[112:115]\n"
    "ds_read_b64_tr_b16 a[154:155], v22 offset:1792\n"
    "v_mfma_f32_16x16x32_bf16 v[144:147], a[176:179], v[56:59], v[144:147]\n"
    "ds_read_b64_tr_b16 a[156:157], v22 offset:1544\n"
    "v_mfma_f32_16x16x32_bf16 v[176:179], a[176:179], v[60:63], v[176:179]\n"
    "ds_read_b64_tr_b16 a[158:159], v22 offset:1800\n"
    "v_mfma_f32_16x16x32_bf16 v[84:87], a[180:183], v[48:51], v[84:87]\n"
    "ds_read_b64_tr_b16 a[192:193], v23 offset:0\n"
    "v_mfma_f32_16x16x32_bf16 v[116:119], a[180:183], v[52:55], v[116:119]\n"
    "ds_read_b64_tr_b16 a[194:195], v23 offset:256\n"
    "v_mfma_f32_16x16x32_bf16 v[148:151], a[180:183], v[56:59], v[148:151]\n"
    "ds_read_b64_tr_b16 a[196:197], v23 offset:8\n"
    "v_mfma_f32_16x16x32_bf16 v[180:183], a[180:183], v[60:63], v[180:183]\n"
    "ds_read_b64_tr_b16 a[198:199], v23 offset:264\n"
    "v_mfma_f32_16x16x32_bf16 v[88:91], a[184:187], v[48:51], v[88:91]\n"
    "s_add_u32 m0, s70, 32768\n"
    "s_add_u32 s74, s80, 32768\n"
    "s_and_b32 s74, s74, 0x7ffff\n"
    "buffer_load_dwordx4 v5, s[40:43], s74 offen offset:0 lds\n"
    "v_mfma_f32_16x16x32_bf16 v[120:123], a[184:187], v[52:55], v[120:123]\n"
    "v_mfma_f32_16x16x32_bf16 v[152:155], a[184:187], v[56:59], v[152:155]\n"
    "v_mfma_f32_16x16x32_bf16 v[184:187], a[184:187], v[60:63], v[184:187]\n"
    "buffer_load_dwordx4 v6, s[40:43], s74 offen offset:1024 lds\n"
    "v_mfma_f32_16x16x32_bf16 v[92:95], a[188:191], v[48:51], v[92:95]\n"
    "v_mfma_f32_16x16x32_bf16 v[124:127], a[188:191], v[52:55], v[124:127]\n"
    "v_mfma_f32_16x16x32_bf16 v[156:159], a[188:191], v[56:59], v[156:159]\n"
    "buffer_load_dwordx4 v7, s[40:43], s74 offen offset:2048 lds\n"
    "v_mfma_f32_16x16x32_bf16 v[188:191], a[188:191], v[60:63], v[188:191]\n"
    "v_mfma_f32_16x16x32_bf16 a[208:211], a[232:235], a[200:203], a[208:211]\n"
    "v_mfma_f32_16x16x32_bf16 a[212:215], a[232:235], a[204:207], a[212:215]\n"
    "buffer_load_dwordx4 v8, s[40:43], s74 offen offset:3072 lds\n"
    "v_mfma_f32_16x16x32_bf16 a[216:219], a[232:235], v[48:51], a[216:219]\n"
    "v_mfma_f32_16x16x32_bf16 a[220:223], a[232:235], v[52:55], a[220:223]\n"
    "v_mfma_f32_16x16x32_bf16 a[224:227], a[232:235], v[56:59], a[224:227]\n"
    "s_add_u32 m0, s71, 16384\n"
    "s_add_u32 s74, s80, 16384\n"
    "s_and_b32 s74, s74, 0x7ffff\n"
    "buffer_load_dwordx4 v9, s[44:47], s74 offen offset:0 lds\n"
    "v_mfma_f32_16x16x32_bf16 a[228:231], a[232:235], v[60:63], a[228:231]\n"
    "v_mfma_f32_16x16x32_bf16 a[236:239], v[48:51], v[48:51], a[236:239]\n"
    "v_mfma_f32_16x16x32_bf16 a[240:243], v[52:55], v[52:55], a[240:243]\n"
    "buffer_load_dwordx4 v10, s[44:47], s74 offen offset:1024 lds\n"
    "v_mfma_f32_16x16x32_bf16 a[244:247], v[56:59], v[56:59], a[244:247]\n"
    "v_mfma_f32_16x16x32_bf16 a[248:251], v[60:63], v[60:63], a[248:251]\n"
    "s_waitcnt lgkmcnt(0)\n"
    "v_mfma_f32_16x16x32_bf16 v[64:67], a[128:131], v[32:35], v[64:67]\n"
    "ds_read_b128 v[48:51], v24 offset:512\n"
    "v_mfma_f32_16x16x32_bf16 v[96:99], a[128:131], v[36:39], v[96:99]\n"
    "ds_read_b128 v[52:55], v24 offset:2560\n"
    "v_mfma_f32_16x16x32_bf16 v[128:131], a[128:131], v[40:43], v[128:131]\n"
    "ds_read_b128 v[56:59], v24 offset:4608\n"
    "v_mfma_f32_16x16x32_bf16 v[160:163], a[128:131], v[44:47], v[160:163]\n"
    "ds_read_b128 v[60:63], v24 offset:6656\n"
    "v_mfma_f32_16x16x32_bf16 v[68:71], a[132:135], v[32:35], v[68:71]\n"
    "ds_read_b64_tr_b16 a[160:161], v22 offset:8192\n"
    "v_mfma_f32_16x16x32_bf16 v[100:103], a[132:135], v[36:39], v[100:103]\n"
    "ds_read_b64_tr_b16 a[162:163], v22 offset:8448\n"
    "v_mfma_f32_16x16x32_bf16 v[132:135], a[132:135], v[40:43], v[132:135]\n"
    "ds_read_b64_tr_b16 a[164:165], v22 offset:8200\n"
    "v_mfma_f32_16x16x32_bf16 v[164:167], a[132:135], v[44:47], v[164:167]\n"
    "ds_read_b64_tr_b16 a[166:167], v22 offset:8456\n"
    "v_mfma_f32_16x16x32_bf16 v[72:75], a[136:139], v[32:35], v[72:75]\n"
    "ds_read_b64_tr_b16 a[168:169], v22 offset:8704\n"
    "v_mfma_f32_16x16x32_bf16 v[104:107], a[136:139], v[36:39], v[104:107]\n"
    "ds_read_b64_tr_b16 a[170:171], v22 offset:8960\n"
    "v_mfma_f32_16x16x32_bf16 v[136:139], a[136:139], v[40:43], v[136:139]\n"
    "ds_read_b64_tr_b16 a[172:173], v22 offset:8712\n"
    "v_mfma_f32_16x16x32_bf16 v[168:171], a[136:139], v[44:47], v[168:171]\n"
    "ds_read_b64_tr_b16 a[174:175], v22 offset:8968\n"
    "v_mfma_f32_16x16x32_bf16 v[76:79], a[140:143], v[32:35], v[76:79]\n"
    "ds_read_b64_tr_b16 a[176:177], v22 offset:9216\n"
    "v_mfma_f32_16x16x32_bf16 v[108:111], a[140:143], v[36:39], v[108:111]\n"
    "ds_read_b64_tr_b16 a[178:179], v22 offset:9472\n"
    "v_mfma_f32_16x16x32_bf16 v[140:143], a[140:143], v[40:43], v[140:143]\n"
    "ds_read_b64_tr_b16 a[180:181], v22 offset:9224\n"
    "v_mfma_f32_16x16x32_bf16 v[172:175], a[140:143], v[44:47], v[172:175]\n"
    "ds_read_b64_tr_b16 a[182:183], v22 offset:9480\n"
    "v_mfma_f32_16x16x32_bf16 v[80:83], a[144:147], v[32:35], v[80:83]\n"
    "ds_read_b64_tr_b16 a[184:185], v22 offset:9728\n"
    "v_mfma_f32_16x16x32_bf16 v[112:115], a[144:147], v[36:39], v[112:115]\n"
    "ds_read_b64_tr_b16 a[186:187], v22 offset:9984\n"
    "v_mfma_f32_16x16x32_bf16 v[144:147], a[144:147], v[40:43], v[144:147]\n"
    "ds_read_b64_tr_b16 a[188:189], v22 offset:9736\n"
    "v_mfma_f32_16x16x32_bf16 v[176:179], a[144:147], v[44:47], v[176:179]\n"
    "ds_read_b64_tr_b16 a[190:191], v22 offset:9992\n"
    "v_mfma_f32_16x16x32_bf16 v[84:87], a[148:151], v[32:35], v[84:87]\n"
    "ds_read_b64_tr_b16 a[200:201], v23 offset:8192\n"
    "v_mfma_f32_16x16x32_bf16 v[116:119], a[148:151], v[36:39], v[116:119]\n"
    "ds_read_b64_tr_b16 a[202:203], v23 offset:8448\n"
    "v_mfma_f32_16x16x32_bf16 v[148:151], a[148:151], v[40:43], v[148:151]\n"
    "ds_read_b64_tr_b16 a[204:205], v23 offset:8200\n"
    "v_mfma_f32_16x16x32_bf16 v[180:183], a[148:151], v[44:47], v[180:183]\n"
    "ds_read_b64_tr_b16 a[206:207], v23 offset:8456\n"
    "v_mfma_f32_16x16x32_bf16 v[88:91], a[152:155], v[32:35], v[88:91]\n"
    "buffer_load_dwordx4 v9, s[44:47], s74 offen offset:2048 lds\n"
    "v_mfma_f32_16x16x32_bf16 v[120:123], a[152:155], v[36:39], v[120:123]\n"
    "v_mfma_f32_16x16x32_bf16 v[152:155], a[152:155], v[40:43], v[152:155]\n"
    "v_mfma_f32_16x16x32_bf16 v[184:187], a[152:155], v[44:47], v[184:187]\n"
    "buffer_load_dwordx4 v10, s[44:47], s74 offen offset:3072 lds\n"
    "v_mfma_f32_16x16x32_bf16 v[92:95], a[156:159], v[32:35], v[92:95]\n"
    "v_mfma_f32_16x16x32_bf16 v[124:127], a[156:159], v[36:39], v[124:127]\n"
    "v_mfma_f32_16x16x32_bf16 v[156:159], a[156:159], v[40:43], v[156:159]\n"
    "s_add_u32 m0, s71, 32768\n"
    "s_add_u32 s74, s80, 32768\n"
    "s_and_b32 s74, s74, 0x7ffff\n"
    "buffer_load_dwordx4 v9, s[44:47], s74 offen offset:0 lds\n"
    "v_mfma_f32_16x16x32_bf16 v[188:191], a[156:159], v[44:47], v[188:191]\n"
    "v_mfma_f32_16x16x32_bf16 a[208:211], a[232:235], a[192:195], a[208:211]\n"
    "v_mfma_f32_16x16x32_bf16 a[212:215], a[232:235], a[196:199], a[212:215]\n"
    "buffer_load_dwordx4 v10, s[44:47], s74 offen offset:1024 lds\n"
    "v_mfma_f32_16x16x32_bf16 a[216:219], a[232:235], v[32:35], a[216:219]\n"
    "v_mfma_f32_16x16x32_bf16 a[220:223], a[232:235], v[36:39], a[220:223]\n"
    "v_mfma_f32_16x16x32_bf16 a[224:227], a[232:235], v[40:43], a[224:227]\n"
    "buffer_load_dwordx4 v9, s[44:47], s74 offen offset:2048 lds\n"
    "v_mfma_f32_16x16x32_bf16 a[228:231], a[232:235], v[44:47], a[228:231]\n"
    "v_mfma_f32_16x16x32_bf16 a[236:239], v[32:35], v[32:35], a[236:239]\n"
    "v_mfma_f32_16x16x32_bf16 a[240:243], v[36:39], v[36:39], a[240:243]\n"
    "buffer_load_dwordx4 v10, s[44:47], s74 offen offset:3072 lds\n"
    "v_mfma_f32_16x16x32_bf16 a[244:247], v[40:43], v[40:43], a[244:247]\n"
    "v_mfma_f32_16x16x32_bf16 a[248:251], v[44:47], v[44:47], a[248:251]\n"
    "s_waitcnt lgkmcnt(0)\n"
    "v_mfma_f32_16x16x32_bf16 v[64:67], a[160:163], v[48:51], v[64:67]\n"
    "v_mfma_f32_16x16x32_bf16 v[96:99], a[160:163], v[52:55], v[96:99]\n"
    "v_mfma_f32_16x16x32_bf16 v[128:131], a[160:163], v[56:59], v[128:131]\n"
    "v_mfma_f32_16x16x32_bf16 v[160:163], a[160:163], v[60:63], v[160:163]\n"
    "v_mfma_f32_16x16x32_bf16 v[68:71], a[164:167], v[48:51], v[68:71]\n"
    "v_mfma_f32_16x16x32_bf16 v[100:103], a[164:167], v[52:55], v[100:103]\n"
    "v_mfma_f32_16x16x32_bf16 v[132:135], a[164:167], v[56:59], v[132:135]\n"
    "v_mfma_f32_16x16x32_bf16 v[164:167], a[164:167], v[60:63], v[164:167]\n"
    "v_mfma_f32_16x16x32_bf16 v[72:75], a[168:171], v[48:51], v[72:75]\n"
    "v_mfma_f32_16x16x32_bf16 v[104:107], a[168:171], v[52:55], v[104:107]\n"
    "v_mfma_f32_16x16x32_bf16 v[136:139], a[168:171], v[56:59], v[136:139]\n"
    "v_mfma_f32_16x16x32_bf16 v[168:171], a[168:171], v[60:63], v[168:171]\n"
    "v_mfma_f32_16x16x32_bf16 v[76:79], a[172:175], v[48:51], v[76:79]\n"
    "v_mfma_f32_16x16x32_bf16 v[108:111], a[172:175], v[52:55], v[108:111]\n"
    "v_mfma_f32_16x16x32_bf16 v[140:143], a[172:175], v[56:59], v[140:143]\n"
    "v_mfma_f32_16x16x32_bf16 v[172:175], a[172:175], v[60:63], v[172:175]\n"
    "v_mfma_f32_16x16x32_bf16 v[80:83], a[176:179], v[48:51], v[80:83]\n"
    "v_mfma_f32_16x16x32_bf16 v[112:115], a[176:179], v[52:55], v[112:115]\n"
    "v_mfma_f32_16x16x32_bf16 v[144:147], a[176:179], v[56:59], v[144:147]\n"
    "v_mfma_f32_16x16x32_bf16 v[176:179], a[176:179], v[60:63], v[176:179]\n"
    "v_mfma_f32_16x16x32_bf16 v[84:87], a[180:183], v[48:51], v[84:87]\n"
    "v_mfma_f32_16x16x32_bf16 v[116:119], a[180:183], v[52:55], v[116:119]\n"
    "v_mfma_f32_16x16x32_bf16 v[148:151], a[180:183], v[56:59], v[148:151]\n"
    "v_mfma_f32_16x16x32_bf16 v[180:183], a[180:183], v[60:63], v[180:183]\n"
    "v_mfma_f32_16x16x32_bf16 v[88:91], a[184:187], v[48:51], v[88:91]\n"
    "v_mfma_f32_16x16x32_bf16 v[120:123], a[184:187], v[52:55], v[120:123]\n"
    "v_mfma_f32_16x16x32_bf16 v[152:155], a[184:187], v[56:59], v[152:155]\n"
    "v_mfma_f32_16x16x32_bf16 v[184:187], a[184:187], v[60:63], v[184:187]\n"
    "v_mfma_f32_16x16x32_bf16 v[92:95], a[188:191], v[48:51], v[92:95]\n"
    "v_mfma_f32_16x16x32_bf16 v[124:127], a[188:191], v[52:55], v[124:127]\n"
    "v_mfma_f32_16x16x32_bf16 v[156:159], a[188:191], v[56:59], v[156:159]\n"
    "v_mfma_f32_16x16x32_bf16 v[188:191], a[188:191], v[60:63], v[188:191]\n"
    "v_mfma_f32_16x16x32_bf16 a[208:211], a[232:235], a[200:203], a[208:211]\n"
    "v_mfma_f32_16x16x32_bf16 a[212:215], a[232:235], a[204:207], a[212:215]\n"
    "v_mfma_f32_16x16x32_bf16 a[216:219], a[232:235], v[48:51], a[216:219]\n"
    "v_mfma_f32_16x16x32_bf16 a[220:223], a[232:235], v[52:55], a[220:223]\n"
    "v_mfma_f32_16x16x32_bf16 a[224:227], a[232:235], v[56:59], a[224:227]\n"
    "v_mfma_f32_16x16x32_bf16 a[228:231], a[232:235], v[60:63], a[228:231]\n"
    "v_mfma_f32_16x16x32_bf16 a[236:239], v[48:51], v[48:51], a[236:239]\n"
    "v_mfma_f32_16x16x32_bf16 a[240:243], v[52:55], v[52:55], a[240:243]\n"
    "v_mfma_f32_16x16x32_bf16 a[244:247], v[56:59], v[56:59], a[244:247]\n"
    "v_mfma_f32_16x16x32_bf16 a[248:251], v[60:63], v[60:63], a[248:251]\n"
    "v_and_b32 v29, 15, v1\n"
    "v_lshlrev_b32 v29, 2, v29\n"
    "s_lshl_b32 s73, s64, 7\n"
    "v_add_u32 v29, 131072, v29\n"
    "v_add_u32 v30, s73, v29\n"
    "v_mov_b32 v31, 0\n"
    "v_accvgpr_read_b32 v15, a208\n"
    "v_accvgpr_read_b32 v16, a212\n"
    "ds_write_b32 v30, v15\n"
    "ds_write_b32 v30, v16 offset:64\n"
    "v_accvgpr_read_b32 v11, a216\n"
    "v_accvgpr_read_b32 v12, a220\n"
    "v_accvgpr_read_b32 v13, a224\n"
    "v_accvgpr_read_b32 v14, a228\n"
    "v_lshrrev_b32 v29, 4, v1\n"
    "v_lshlrev_b32 v29, 2, v29\n"
    "v_and_b32 v31, 15, v1\n"
    "v_sub_u32 v29, v31, v29\n"
    "v_accvgpr_read_b32 v32, a236\n"
    "v_accvgpr_read_b32 v33, a237\n"
    "v_accvgpr_read_b32 v34, a238\n"
    "v_accvgpr_read_b32 v35, a239\n"
    "v_accvgpr_read_b32 v36, a240\n"
    "v_accvgpr_read_b32 v37, a241\n"
    "v_accvgpr_read_b32 v38, a242\n"
    "v_accvgpr_read_b32 v39, a243\n"
    "v_accvgpr_read_b32 v40, a244\n"
    "v_accvgpr_read_b32 v41, a245\n"
    "v_accvgpr_read_b32 v42, a246\n"
    "v_accvgpr_read_b32 v43, a247\n"
    "v_accvgpr_read_b32 v44, a248\n"
    "v_accvgpr_read_b32 v45, a249\n"
    "v_accvgpr_read_b32 v46, a250\n"
    "v_accvgpr_read_b32 v47, a251\n"
    "v_cmp_eq_u32 vcc, 0, v29\n"
    "s_nop 1\n"
    "v_cndmask_b32 v25, 0, v32, vcc\n"
    "v_cndmask_b32 v26, 0, v36, vcc\n"
    "v_cndmask_b32 v27, 0, v40, vcc\n"
    "v_cndmask_b32 v28, 0, v44, vcc\n"
    "v_cmp_eq_u32 vcc, 1, v29\n"
    "s_nop 1\n"
    "v_cndmask_b32 v25, v25, v33, vcc\n"
    "v_cndmask_b32 v26, v26, v37, vcc\n"
    "v_cndmask_b32 v27, v27, v41, vcc\n"
    "v_cndmask_b32 v28, v28, v45, vcc\n"
    "v_cmp_eq_u32 vcc, 2, v29\n"
    "s_nop 1\n"
    "v_cndmask_b32 v25, v25, v34, vcc\n"
    "v_cndmask_b32 v26, v26, v38, vcc\n"
    "v_cndmask_b32 v27, v27, v42, vcc\n"
    "v_cndmask_b32 v28, v28, v46, vcc\n"
    "v_cmp_eq_u32 vcc, 3, v29\n"
    "s_nop 1\n"
    "v_cndmask_b32 v25, v25, v35, vcc\n"
    "v_cndmask_b32 v26, v26, v39, vcc\n"
    "v_cndmask_b32 v27, v27, v43, vcc\n"
    "v_cndmask_b32 v28, v28, v47, vcc\n"
    "v_mov_b32 v48, v25\n"
    "v_mov_b32 v49, v26\n"
    "v_mov_b32 v50, v27\n"
    "v_mov_b32 v51, v28\n"
    "s_nop 1\n"
    "v_permlane16_swap_b32 v25, v48\n"
    "v_permlane16_swap_b32 v26, v49\n"
    "v_permlane16_swap_b32 v27, v50\n"
    "v_permlane16_swap_b32 v28, v51\n"
    "v_add_f32 v25, v25, v48\n"
    "v_add_f32 v26, v26, v49\n"
    "v_add_f32 v27, v27, v50\n"
    "v_add_f32 v28, v28, v51\n"
    "v_mov_b32 v48, v25\n"
    "v_mov_b32 v49, v26\n"
    "v_mov_b32 v50, v27\n"
    "v_mov_b32 v51, v28\n"
    "s_nop 1\n"
    "v_permlane32_swap_b32 v25, v48\n"
    "v_permlane32_swap_b32 v26, v49\n"
    "v_permlane32_swap_b32 v27, v50\n"
    "v_permlane32_swap_b32 v28, v51\n"
    "v_add_f32 v25, v25, v48\n"
    "v_add_f32 v26, v26, v49\n"
    "v_add_f32 v27, v27, v50\n"
    "v_add_f32 v28, v28, v51\n"
    "s_waitcnt vmcnt(12)\n"
    "s_waitcnt lgkmcnt(0)\n"
    "s_barrier\n"
    "v_lshrrev_b32 v29, 4, v1\n"
    "v_lshlrev_b32 v29, 4, v29\n"
    "v_add_u32 v29, 131072, v29\n"
    "ds_read_b128 v[32:35], v29 offset:0\n"
    "ds_read_b128 v[36:39], v29 offset:64\n"
    "ds_read_b128 v[40:43], v29 offset:128\n"
    "ds_read_b128 v[44:47], v29 offset:192\n"
    "ds_read_b128 v[48:51], v29 offset:256\n"
    "ds_read_b128 v[52:55], v29 offset:320\n"
    "ds_read_b128 v[56:59], v29 offset:384\n"
    "ds_read_b128 v[60:63], v29 offset:448\n"
    "ds_read_b128 a[128:131], v2 offset:0\n"
    "ds_read_b128 a[132:135], v2 offset:512\n"
    "ds_read_b128 a[136:139], v2 offset:1024\n"
    "ds_read_b128 a[140:143], v2 offset:1536\n"
    "ds_read_b128 a[144:147], v2 offset:4096\n"
    "ds_read_b128 a[148:151], v2 offset:4608\n"
    "ds_read_b128 a[152:155], v2 offset:5120\n"
    "ds_read_b128 a[156:159], v2 offset:5632\n"
    "s_lshl_b32 s73, s65, 17\n"
    "s_lshl_b32 s74, s64, 15\n"
    "s_add_u32 s73, s73, s74\n"
    "s_lshl_b32 s74, s66, 7\n"
    "s_add_u32 s73, s73, s74\n"
    "v_bfe_u32 v30, v1, 2, 3\n"
    "v_lshlrev_b32 v30, 10, v30\n"
    "v_and_b32 v31, 0xff, v9\n"
    "v_add_u32 v30, v31, v30\n"
    "s_add_u32 m0, s70, 147456\n"
    "s_add_u32 s74, s73, 0\n"
    "buffer_load_dwordx4 v30, s[48:51], s74 offen offset:0 lds\n"
    "s_add_u32 s74, s73, 7168\n"
    "buffer_load_dwordx4 v30, s[48:51], s74 offen offset:1024 lds\n"
    "s_add_u32 s74, s73, 14336\n"
    "buffer_load_dwordx4 v30, s[48:51], s74 offen offset:2048 lds\n"
    "s_add_u32 s74, s73, 21504\n"
    "buffer_load_dwordx4 v30, s[48:51], s74 offen offset:3072 lds\n"
    "v_mul_f32 v192, 0x3b000000, v11\n"
    "v_mul_f32 v196, 0x3b000000, v12\n"
    "v_mul_f32 v200, 0x3b000000, v13\n"
    "v_mul_f32 v204, 0x3b000000, v14\n"
    "v_mul_f32 v193, 0x3b000000, v25\n"
    "v_mul_f32 v197, 0x3b000000, v26\n"
    "v_mul_f32 v201, 0x3b000000, v27\n"
    "v_mul_f32 v205, 0x3b000000, v28\n"
    "v_fma_f32 v193, -v192, v192, v193\n"
    "v_fma_f32 v197, -v196, v196, v197\n"
    "v_fma_f32 v201, -v200, v200, v201\n"
    "v_fma_f32 v205, -v204, v204, v205\n"
    "v_max_f32 v193, 0, v193\n"
    "v_max_f32 v197, 0, v197\n"
    "v_max_f32 v201, 0, v201\n"
    "v_max_f32 v205, 0, v205\n"
    "v_cvt_pk_bf16_f32 v194, v192, v193\n"
    "v_cvt_pk_bf16_f32 v198, v196, v197\n"
    "v_cvt_pk_bf16_f32 v202, v200, v201\n"
    "v_cvt_pk_bf16_f32 v206, v204, v205\n"
    "v_lshlrev_b32 v192, 16, v194\n"
    "v_lshlrev_b32 v196, 16, v198\n"
    "v_lshlrev_b32 v200, 16, v202\n"
    "v_lshlrev_b32 v204, 16, v206\n"
    "v_and_b32 v193, 0xffff0000, v194\n"
    "v_and_b32 v197, 0xffff0000, v198\n"
    "v_and_b32 v201, 0xffff0000, v202\n"
    "v_and_b32 v205, 0xffff0000, v206\n"
    "v_add_f32 v193, 0x3727c5ac, v193\n"
    "v_add_f32 v197, 0x3727c5ac, v197\n"
    "v_add_f32 v201, 0x3727c5ac, v201\n"
    "v_add_f32 v205, 0x3727c5ac, v205\n"
    "v_cvt_pk_bf16_f32 v193, v193, v193\n"
    "v_cvt_pk_bf16_f32 v197, v197, v197\n"
    "v_cvt_pk_bf16_f32 v201, v201, v201\n"
    "v_cvt_pk_bf16_f32 v205, v205, v205\n"
    "v_lshlrev_b32 v193, 16, v193\n"
    "v_lshlrev_b32 v197, 16, v197\n"
    "v_lshlrev_b32 v201, 16, v201\n"
    "v_lshlrev_b32 v205, 16, v205\n"
    "v_sqrt_f32 v193, v193\n"
    "v_sqrt_f32 v197, v197\n"
    "v_sqrt_f32 v201, v201\n"
    "v_sqrt_f32 v205, v205\n"
    "v_cvt_pk_bf16_f32 v193, v193, v193\n"
    "v_cvt_pk_bf16_f32 v197, v197, v197\n"
    "v_cvt_pk_bf16_f32 v201, v201, v201\n"
    "v_cvt_pk_bf16_f32 v205, v205, v205\n"
    "v_lshlrev_b32 v193, 16, v193\n"
    "v_lshlrev_b32 v197, 16, v197\n"
    "v_lshlrev_b32 v201, 16, v201\n"
    "v_lshlrev_b32 v205, 16, v205\n"
    "v_rcp_f32 v194, v193\n"
    "v_rcp_f32 v198, v197\n"
    "v_rcp_f32 v202, v201\n"
    "v_rcp_f32 v206, v205\n"
    "v_mul_f32 v30, 0x3e0293ee, v194\n"
    "v_mul_f32 v18, 0x3e0293ee, v198\n"
    "v_mul_f32 v20, 0x3e0293ee, v202\n"
    "v_mul_f32 v22, 0x3e0293ee, v206\n"
    "v_sub_f32 v24, 0, v192\n"
    "v_sub_f32 v26, 0, v196\n"
    "v_sub_f32 v28, 0, v200\n"
    "v_sub_f32 v14, 0, v204\n"
    "s_waitcnt lgkmcnt(8)\n"
    "v_pk_fma_f32 v[64:65], v[32:33], v[24:25], v[64:65] op_sel_hi:[1,0,1]\n"
    "v_pk_fma_f32 v[66:67], v[34:35], v[24:25], v[66:67] op_sel_hi:[1,0,1]\n"
    "v_pk_fma_f32 v[68:69], v[36:37], v[24:25], v[68:69] op_sel_hi:[1,0,1]\n"
    "v_pk_fma_f32 v[70:71], v[38:39], v[24:25], v[70:71] op_sel_hi:[1,0,1]\n"
    "v_pk_fma_f32 v[72:73], v[40:41], v[24:25], v[72:73] op_sel_hi:[1,0,1]\n"
    "v_pk_fma_f32 v[74:75], v[42:43], v[24:25], v[74:75] op_sel_hi:[1,0,1]\n"
    "v_pk_fma_f32 v[76:77], v[44:45], v[24:25], v[76:77] op_sel_hi:[1,0,1]\n"
    "v_pk_fma_f32 v[78:79], v[46:47], v[24:25], v[78:79] op_sel_hi:[1,0,1]\n"
    "v_pk_fma_f32 v[80:81], v[48:49], v[24:25], v[80:81] op_sel_hi:[1,0,1]\n"
    "v_pk_fma_f32 v[82:83], v[50:51], v[24:25], v[82:83] op_sel_hi:[1,0,1]\n"
    "v_pk_fma_f32 v[84:85], v[52:53], v[24:25], v[84:85] op_sel_hi:[1,0,1]\n"
    "v_pk_fma_f32 v[86:87], v[54:55], v[24:25], v[86:87] op_sel_hi:[1,0,1]\n"
    "v_pk_fma_f32 v[88:89], v[56:57], v[24:25], v[88:89] op_sel_hi:[1,0,1]\n"
    "v_pk_fma_f32 v[90:91], v[58:59], v[24:25], v[90:91] op_sel_hi:[1,0,1]\n"
    "v_pk_fma_f32 v[92:93], v[60:61], v[24:25], v[92:93] op_sel_hi:[1,0,1]\n"
    "v_pk_fma_f32 v[94:95], v[62:63], v[24:25], v[94:95] op_sel_hi:[1,0,1]\n"
    "v_pk_mul_f32 v[64:65], v[64:65], v[30:31] op_sel_hi:[1,0]\n"
    "v_pk_mul_f32 v[66:67], v[66:67], v[30:31] op_sel_hi:[1,0]\n"
    "v_pk_mul_f32 v[68:69], v[68:69], v[30:31] op_sel_hi:[1,0]\n"
    "v_pk_mul_f32 v[70:71], v[70:71], v[30:31] op_sel_hi:[1,0]\n"
    "v_pk_mul_f32 v[72:73], v[72:73], v[30:31] op_sel_hi:[1,0]\n"
    "v_pk_mul_f32 v[74:75], v[74:75], v[30:31] op_sel_hi:[1,0]\n"
    "v_pk_mul_f32 v[76:77], v[76:77], v[30:31] op_sel_hi:[1,0]\n"
    "v_pk_mul_f32 v[78:79], v[78:79], v[30:31] op_sel_hi:[1,0]\n"
    "v_pk_mul_f32 v[80:81], v[80:81], v[30:31] op_sel_hi:[1,0]\n"
    "v_pk_mul_f32 v[82:83], v[82:83], v[30:31] op_sel_hi:[1,0]\n"
    "v_pk_mul_f32 v[84:85], v[84:85], v[30:31] op_sel_hi:[1,0]\n"
    "v_pk_mul_f32 v[86:87], v[86:87], v[30:31] op_sel_hi:[1,0]\n"
    "v_pk_mul_f32 v[88:89], v[88:89], v[30:31] op_sel_hi:[1,0]\n"
    "v_pk_mul_f32 v[90:91], v[90:91], v[30:31] op_sel_hi:[1,0]\n"
    "v_pk_mul_f32 v[92:93], v[92:93], v[30:31] op_sel_hi:[1,0]\n"
    "v_pk_mul_f32 v[94:95], v[94:95], v[30:31] op_sel_hi:[1,0]\n"
    "v_cvt_pk_bf16_f32 v192, v64, v65\n"
    "v_cvt_pk_bf16_f32 v193, v66, v67\n"
    "v_cvt_pk_bf16_f32 v194, v68, v69\n"
    "v_cvt_pk_bf16_f32 v195, v70, v71\n"
    "v_cvt_pk_bf16_f32 v196, v72, v73\n"
    "v_cvt_pk_bf16_f32 v197, v74, v75\n"
    "v_cvt_pk_bf16_f32 v198, v76, v77\n"
    "v_cvt_pk_bf16_f32 v199, v78, v79\n"
    "v_cvt_pk_bf16_f32 v200, v80, v81\n"
    "v_cvt_pk_bf16_f32 v201, v82, v83\n"
    "v_cvt_pk_bf16_f32 v202, v84, v85\n"
    "v_cvt_pk_bf16_f32 v203, v86, v87\n"
    "v_cvt_pk_bf16_f32 v204, v88, v89\n"
    "v_cvt_pk_bf16_f32 v205, v90, v91\n"
    "v_cvt_pk_bf16_f32 v206, v92, v93\n"
    "v_cvt_pk_bf16_f32 v207, v94, v95\n"
    "v_pk_fma_f32 v[96:97], v[32:33], v[26:27], v[96:97] op_sel_hi:[1,0,1]\n"
    "v_pk_fma_f32 v[98:99], v[34:35], v[26:27], v[98:99] op_sel_hi:[1,0,1]\n"
    "v_pk_fma_f32 v[100:101], v[36:37], v[26:27], v[100:101] op_sel_hi:[1,0,1]\n"
    "v_pk_fma_f32 v[102:103], v[38:39], v[26:27], v[102:103] op_sel_hi:[1,0,1]\n"
    "v_pk_fma_f32 v[104:105], v[40:41], v[26:27], v[104:105] op_sel_hi:[1,0,1]\n"
    "v_pk_fma_f32 v[106:107], v[42:43], v[26:27], v[106:107] op_sel_hi:[1,0,1]\n"
    "v_pk_fma_f32 v[108:109], v[44:45], v[26:27], v[108:109] op_sel_hi:[1,0,1]\n"
    "v_pk_fma_f32 v[110:111], v[46:47], v[26:27], v[110:111] op_sel_hi:[1,0,1]\n"
    "v_pk_fma_f32 v[112:113], v[48:49], v[26:27], v[112:113] op_sel_hi:[1,0,1]\n"
    "v_pk_fma_f32 v[114:115], v[50:51], v[26:27], v[114:115] op_sel_hi:[1,0,1]\n"
    "v_pk_fma_f32 v[116:117], v[52:53], v[26:27], v[116:117] op_sel_hi:[1,0,1]\n"
    "v_pk_fma_f32 v[118:119], v[54:55], v[26:27], v[118:119] op_sel_hi:[1,0,1]\n"
    "v_pk_fma_f32 v[120:121], v[56:57], v[26:27], v[120:121] op_sel_hi:[1,0,1]\n"
    "v_pk_fma_f32 v[122:123], v[58:59], v[26:27], v[122:123] op_sel_hi:[1,0,1]\n"
    "v_pk_fma_f32 v[124:125], v[60:61], v[26:27], v[124:125] op_sel_hi:[1,0,1]\n"
    "v_pk_fma_f32 v[126:127], v[62:63], v[26:27], v[126:127] op_sel_hi:[1,0,1]\n"
    "v_pk_mul_f32 v[96:97], v[96:97], v[18:19] op_sel_hi:[1,0]\n"
    "v_pk_mul_f32 v[98:99], v[98:99], v[18:19] op_sel_hi:[1,0]\n"
    "v_pk_mul_f32 v[100:101], v[100:101], v[18:19] op_sel_hi:[1,0]\n"
    "v_pk_mul_f32 v[102:103], v[102:103], v[18:19] op_sel_hi:[1,0]\n"
    "v_pk_mul_f32 v[104:105], v[104:105], v[18:19] op_sel_hi:[1,0]\n"
    "v_pk_mul_f32 v[106:107], v[106:107], v[18:19] op_sel_hi:[1,0]\n"
    "v_pk_mul_f32 v[108:109], v[108:109], v[18:19] op_sel_hi:[1,0]\n"
    "v_pk_mul_f32 v[110:111], v[110:111], v[18:19] op_sel_hi:[1,0]\n"
    "v_pk_mul_f32 v[112:113], v[112:113], v[18:19] op_sel_hi:[1,0]\n"
    "v_pk_mul_f32 v[114:115], v[114:115], v[18:19] op_sel_hi:[1,0]\n"
    "v_pk_mul_f32 v[116:117], v[116:117], v[18:19] op_sel_hi:[1,0]\n"
    "v_pk_mul_f32 v[118:119], v[118:119], v[18:19] op_sel_hi:[1,0]\n"
    "v_pk_mul_f32 v[120:121], v[120:121], v[18:19] op_sel_hi:[1,0]\n"
    "v_pk_mul_f32 v[122:123], v[122:123], v[18:19] op_sel_hi:[1,0]\n"
    "v_pk_mul_f32 v[124:125], v[124:125], v[18:19] op_sel_hi:[1,0]\n"
    "v_pk_mul_f32 v[126:127], v[126:127], v[18:19] op_sel_hi:[1,0]\n"
    "v_cvt_pk_bf16_f32 v208, v96, v97\n"
    "v_cvt_pk_bf16_f32 v209, v98, v99\n"
    "v_cvt_pk_bf16_f32 v210, v100, v101\n"
    "v_cvt_pk_bf16_f32 v211, v102, v103\n"
    "v_cvt_pk_bf16_f32 v212, v104, v105\n"
    "v_cvt_pk_bf16_f32 v213, v106, v107\n"
    "v_cvt_pk_bf16_f32 v214, v108, v109\n"
    "v_cvt_pk_bf16_f32 v215, v110, v111\n"
    "v_cvt_pk_bf16_f32 v216, v112, v113\n"
    "v_cvt_pk_bf16_f32 v217, v114, v115\n"
    "v_cvt_pk_bf16_f32 v218, v116, v117\n"
    "v_cvt_pk_bf16_f32 v219, v118, v119\n"
    "v_cvt_pk_bf16_f32 v220, v120, v121\n"
    "v_cvt_pk_bf16_f32 v221, v122, v123\n"
    "v_cvt_pk_bf16_f32 v222, v124, v125\n"
    "v_cvt_pk_bf16_f32 v223, v126, v127\n"
    "v_pk_fma_f32 v[128:129], v[32:33], v[28:29], v[128:129] op_sel_hi:[1,0,1]\n"
    "v_pk_fma_f32 v[130:131], v[34:35], v[28:29], v[130:131] op_sel_hi:[1,0,1]\n"
    "v_pk_fma_f32 v[132:133], v[36:37], v[28:29], v[132:133] op_sel_hi:[1,0,1]\n"
    "v_pk_fma_f32 v[134:135], v[38:39], v[28:29], v[134:135] op_sel_hi:[1,0,1]\n"
    "v_pk_fma_f32 v[136:137], v[40:41], v[28:29], v[136:137] op_sel_hi:[1,0,1]\n"
    "v_pk_fma_f32 v[138:139], v[42:43], v[28:29], v[138:139] op_sel_hi:[1,0,1]\n"
    "v_pk_fma_f32 v[140:141], v[44:45], v[28:29], v[140:141] op_sel_hi:[1,0,1]\n"
    "v_pk_fma_f32 v[142:143], v[46:47], v[28:29], v[142:143] op_sel_hi:[1,0,1]\n"
    "v_pk_fma_f32 v[144:145], v[48:49], v[28:29], v[144:145] op_sel_hi:[1,0,1]\n"
    "v_pk_fma_f32 v[146:147], v[50:51], v[28:29], v[146:147] op_sel_hi:[1,0,1]\n"
    "v_pk_fma_f32 v[148:149], v[52:53], v[28:29], v[148:149] op_sel_hi:[1,0,1]\n"
    "v_pk_fma_f32 v[150:151], v[54:55], v[28:29], v[150:151] op_sel_hi:[1,0,1]\n"
    "v_pk_fma_f32 v[152:153], v[56:57], v[28:29], v[152:153] op_sel_hi:[1,0,1]\n"
    "v_pk_fma_f32 v[154:155], v[58:59], v[28:29], v[154:155] op_sel_hi:[1,0,1]\n"
    "v_pk_fma_f32 v[156:157], v[60:61], v[28:29], v[156:157] op_sel_hi:[1,0,1]\n"
    "v_pk_fma_f32 v[158:159], v[62:63], v[28:29], v[158:159] op_sel_hi:[1,0,1]\n"
    "v_pk_mul_f32 v[128:129], v[128:129], v[20:21] op_sel_hi:[1,0]\n"
    "v_pk_mul_f32 v[130:131], v[130:131], v[20:21] op_sel_hi:[1,0]\n"
    "v_pk_mul_f32 v[132:133], v[132:133], v[20:21] op_sel_hi:[1,0]\n"
    "v_pk_mul_f32 v[134:135], v[134:135], v[20:21] op_sel_hi:[1,0]\n"
    "v_pk_mul_f32 v[136:137], v[136:137], v[20:21] op_sel_hi:[1,0]\n"
    "v_pk_mul_f32 v[138:139], v[138:139], v[20:21] op_sel_hi:[1,0]\n"
    "v_pk_mul_f32 v[140:141], v[140:141], v[20:21] op_sel_hi:[1,0]\n"
    "v_pk_mul_f32 v[142:143], v[142:143], v[20:21] op_sel_hi:[1,0]\n"
    "v_pk_mul_f32 v[144:145], v[144:145], v[20:21] op_sel_hi:[1,0]\n"
    "v_pk_mul_f32 v[146:147], v[146:147], v[20:21] op_sel_hi:[1,0]\n"
    "v_pk_mul_f32 v[148:149], v[148:149], v[20:21] op_sel_hi:[1,0]\n"
    "v_pk_mul_f32 v[150:151], v[150:151], v[20:21] op_sel_hi:[1,0]\n"
    "v_pk_mul_f32 v[152:153], v[152:153], v[20:21] op_sel_hi:[1,0]\n"
    "v_pk_mul_f32 v[154:155], v[154:155], v[20:21] op_sel_hi:[1,0]\n"
    "v_pk_mul_f32 v[156:157], v[156:157], v[20:21] op_sel_hi:[1,0]\n"
    "v_pk_mul_f32 v[158:159], v[158:159], v[20:21] op_sel_hi:[1,0]\n"
    "v_cvt_pk_bf16_f32 v224, v128, v129\n"
    "v_cvt_pk_bf16_f32 v225, v130, v131\n"
    "v_cvt_pk_bf16_f32 v226, v132, v133\n"
    "v_cvt_pk_bf16_f32 v227, v134, v135\n"
    "v_cvt_pk_bf16_f32 v228, v136, v137\n"
    "v_cvt_pk_bf16_f32 v229, v138, v139\n"
    "v_cvt_pk_bf16_f32 v230, v140, v141\n"
    "v_cvt_pk_bf16_f32 v231, v142, v143\n"
    "v_cvt_pk_bf16_f32 v232, v144, v145\n"
    "v_cvt_pk_bf16_f32 v233, v146, v147\n"
    "v_cvt_pk_bf16_f32 v234, v148, v149\n"
    "v_cvt_pk_bf16_f32 v235, v150, v151\n"
    "v_cvt_pk_bf16_f32 v236, v152, v153\n"
    "v_cvt_pk_bf16_f32 v237, v154, v155\n"
    "v_cvt_pk_bf16_f32 v238, v156, v157\n"
    "v_cvt_pk_bf16_f32 v239, v158, v159\n"
    "v_pk_fma_f32 v[160:161], v[32:33], v[14:15], v[160:161] op_sel_hi:[1,0,1]\n"
    "v_pk_fma_f32 v[162:163], v[34:35], v[14:15], v[162:163] op_sel_hi:[1,0,1]\n"
    "v_pk_fma_f32 v[164:165], v[36:37], v[14:15], v[164:165] op_sel_hi:[1,0,1]\n"
    "v_pk_fma_f32 v[166:167], v[38:39], v[14:15], v[166:167] op_sel_hi:[1,0,1]\n"
    "v_pk_fma_f32 v[168:169], v[40:41], v[14:15], v[168:169] op_sel_hi:[1,0,1]\n"
    "v_pk_fma_f32 v[170:171], v[42:43], v[14:15], v[170:171] op_sel_hi:[1,0,1]\n"
    "v_pk_fma_f32 v[172:173], v[44:45], v[14:15], v[172:173] op_sel_hi:[1,0,1]\n"
    "v_pk_fma_f32 v[174:175], v[46:47], v[14:15], v[174:175] op_sel_hi:[1,0,1]\n"
    "v_pk_fma_f32 v[176:177], v[48:49], v[14:15], v[176:177] op_sel_hi:[1,0,1]\n"
    "v_pk_fma_f32 v[178:179], v[50:51], v[14:15], v[178:179] op_sel_hi:[1,0,1]\n"
    "v_pk_fma_f32 v[180:181], v[52:53], v[14:15], v[180:181] op_sel_hi:[1,0,1]\n"
    "v_pk_fma_f32 v[182:183], v[54:55], v[14:15], v[182:183] op_sel_hi:[1,0,1]\n"
    "v_pk_fma_f32 v[184:185], v[56:57], v[14:15], v[184:185] op_sel_hi:[1,0,1]\n"
    "v_pk_fma_f32 v[186:187], v[58:59], v[14:15], v[186:187] op_sel_hi:[1,0,1]\n"
    "v_pk_fma_f32 v[188:189], v[60:61], v[14:15], v[188:189] op_sel_hi:[1,0,1]\n"
    "v_pk_fma_f32 v[190:191], v[62:63], v[14:15], v[190:191] op_sel_hi:[1,0,1]\n"
    "v_pk_mul_f32 v[160:161], v[160:161], v[22:23] op_sel_hi:[1,0]\n"
    "v_pk_mul_f32 v[162:163], v[162:163], v[22:23] op_sel_hi:[1,0]\n"
    "v_pk_mul_f32 v[164:165], v[164:165], v[22:23] op_sel_hi:[1,0]\n"
    "v_pk_mul_f32 v[166:167], v[166:167], v[22:23] op_sel_hi:[1,0]\n"
    "v_pk_mul_f32 v[168:169], v[168:169], v[22:23] op_sel_hi:[1,0]\n"
    "v_pk_mul_f32 v[170:171], v[170:171], v[22:23] op_sel_hi:[1,0]\n"
    "v_pk_mul_f32 v[172:173], v[172:173], v[22:23] op_sel_hi:[1,0]\n"
    "v_pk_mul_f32 v[174:175], v[174:175], v[22:23] op_sel_hi:[1,0]\n"
    "v_pk_mul_f32 v[176:177], v[176:177], v[22:23] op_sel_hi:[1,0]\n"
    "v_pk_mul_f32 v[178:179], v[178:179], v[22:23] op_sel_hi:[1,0]\n"
    "v_pk_mul_f32 v[180:181], v[180:181], v[22:23] op_sel_hi:[1,0]\n"
    "v_pk_mul_f32 v[182:183], v[182:183], v[22:23] op_sel_hi:[1,0]\n"
    "v_pk_mul_f32 v[184:185], v[184:185], v[22:23] op_sel_hi:[1,0]\n"
    "v_pk_mul_f32 v[186:187], v[186:187], v[22:23] op_sel_hi:[1,0]\n"
    "v_pk_mul_f32 v[188:189], v[188:189], v[22:23] op_sel_hi:[1,0]\n"
    "v_pk_mul_f32 v[190:191], v[190:191], v[22:23] op_sel_hi:[1,0]\n"
    "v_cvt_pk_bf16_f32 v240, v160, v161\n"
    "v_cvt_pk_bf16_f32 v241, v162, v163\n"
    "v_cvt_pk_bf16_f32 v242, v164, v165\n"
    "v_cvt_pk_bf16_f32 v243, v166, v167\n"
    "v_cvt_pk_bf16_f32 v244, v168, v169\n"
    "v_cvt_pk_bf16_f32 v245, v170, v171\n"
    "v_cvt_pk_bf16_f32 v246, v172, v173\n"
    "v_cvt_pk_bf16_f32 v247, v174, v175\n"
    "v_cvt_pk_bf16_f32 v248, v176, v177\n"
    "v_cvt_pk_bf16_f32 v249, v178, v179\n"
    "v_cvt_pk_bf16_f32 v250, v180, v181\n"
    "v_cvt_pk_bf16_f32 v251, v182, v183\n"
    "v_cvt_pk_bf16_f32 v252, v184, v185\n"
    "v_cvt_pk_bf16_f32 v253, v186, v187\n"
    "v_cvt_pk_bf16_f32 v254, v188, v189\n"
    "v_cvt_pk_bf16_f32 v255, v190, v191\n"
    "s_barrier\n"
    "s_lshl_b32 s73, %7, 2\n"
    "s_add_u32 s73, s73, s64\n"
    "s_min_u32 s31, s73, 703\n"
    "s_bitcmp1_b32 s31, 0\n"
    "s_cselect_b64 s[48:49], %11, %10\n"
    "s_mov_b32 s50, 1409024\n"
    "s_bfe_u32 s73, s31, 0x40001\n"
    "s_mul_i32 s73, s73, 88064\n"
    "s_lshr_b32 s74, s31, 5\n"
    "s_lshl_b32 s74, s74, 7\n"
    "s_add_u32 s73, s73, s74\n"
    "v_lshrrev_b32 v29, 3, v1\n"
    "v_mul_u32_u24 v29, 2752, v29\n"
    "v_and_b32 v30, 7, v1\n"
    "v_lshl_add_u32 v29, v30, 4, v29\n"
    "s_add_u32 m0, s70, 131072\n"
    "s_add_u32 s74, s73, 0\n"
    "buffer_load_dwordx4 v29, s[48:51], s74 offen offset:0 lds\n"
    "s_add_u32 s74, s73, 20992\n"
    "buffer_load_dwordx4 v29, s[48:51], s74 offen offset:1024 lds\n"
    "s_add_u32 s74, s73, 41984\n"
    "buffer_load_dwordx4 v29, s[48:51], s74 offen offset:2048 lds\n"
    "s_add_u32 s74, s73, 62976\n"
    "buffer_load_dwordx4 v29, s[48:51], s74 offen offset:3072 lds\n"
    "v_lshrrev_b32 v29, 5, v1\n"
    "v_lshlrev_b32 v29, 10, v29\n"
    "v_sub_u32 v29, s70, v29\n"
    "v_add_u32 v29, 81920, v29\n"
    "v_add_u32 v30, v29, v4\n"
    "v_add_u32 v29, v29, v3\n"
    "v_mov_b32 v31, 0\n"
    "v_mov_b32 v28, 147456\n"
    "s_waitcnt vmcnt(4)\n"
    "ds_read_b64_tr_b16 a[192:193], v29 offset:0\n"
    "ds_read_b64_tr_b16 a[194:195], v29 offset:2048\n"
    "ds_read_b64_tr_b16 a[196:197], v30 offset:0\n"
    "ds_read_b64_tr_b16 a[198:199], v30 offset:2048\n"
    "ds_read_b64_tr_b16 a[200:201], v29 offset:512\n"
    "ds_read_b64_tr_b16 a[202:203], v29 offset:2560\n"
    "ds_read_b64_tr_b16 a[204:205], v30 offset:512\n"
    "ds_read_b64_tr_b16 a[206:207], v30 offset:2560\n"
    "s_cmp_lg_u32 s64, 0\n"
    "s_cbranch_scc1 LNOZ%=\n"
    "ds_write_b32 v28, v31\n"
    "LNOZ%=:\n"
    "LSTART%=:\n"
    "v_accvgpr_write_b32 a208, 0\n"
    "v_accvgpr_write_b32 a209, 0\n"
    "v_accvgpr_write_b32 a210, 0\n"
    "v_accvgpr_write_b32 a211, 0\n"
    "v_accvgpr_write_b32 a212, 0\n"
    "v_accvgpr_write_b32 a213, 0\n"
    "v_accvgpr_write_b32 a214, 0\n"
    "v_accvgpr_write_b32 a215, 0\n"
    "v_accvgpr_write_b32 a216, 0\n"
    "v_accvgpr_write_b32 a217, 0\n"
    "v_accvgpr_write_b32 a218, 0\n"
    "v_accvgpr_write_b32 a219, 0\n"
    "v_accvgpr_write_b32 a220, 0\n"
    "v_accvgpr_write_b32 a221, 0\n"
    "v_accvgpr_write_b32 a222, 0\n"
    "v_accvgpr_write_b32 a223, 0\n"
    "s_add_u32 s68, s80, 32768\n"
    "s_mov_b32 s67, 8\n"
    "s_waitcnt lgkmcnt(0)\n"
    "v_mfma_f32_16x16x32_bf16 v[48:51], a[128:131], v[192:195], 0\n"
    "v_mfma_f32_16x16x32_bf16 v[48:51], a[132:135], v[196:199], v[48:51]\n"
    "v_mfma_f32_16x16x32_bf16 v[48:51], a[136:139], v[200:203], v[48:51]\n"
    "v_mfma_f32_16x16x32_bf16 v[48:51], a[140:143], v[204:207], v[48:51]\n"
    "v_mfma_f32_16x16x32_bf16 v[52:55], a[144:147], v[192:195], 0\n"
    "v_mfma_f32_16x16x32_bf16 v[52:55], a[148:151], v[196:199], v[52:55]\n"
    "v_mfma_f32_16x16x32_bf16 v[52:55], a[152:155], v[200:203], v[52:55]\n"
    "v_mfma_f32_16x16x32_bf16 v[52:55], a[156:159], v[204:207], v[52:55]\n"
    "v_mfma_f32_16x16x32_bf16 v[56:59], a[128:131], v[208:211], 0\n"
    "v_mfma_f32_16x16x32_bf16 v[56:59], a[132:135], v[212:215], v[56:59]\n"
    "v_mfma_f32_16x16x32_bf16 v[56:59], a[136:139], v[216:219], v[56:59]\n"
    "v_mfma_f32_16x16x32_bf16 v[56:59], a[140:143], v[220:223], v[56:59]\n"
    "v_mfma_f32_16x16x32_bf16 v[60:63], a[144:147], v[208:211], 0\n"
    "v_mfma_f32_16x16x32_bf16 v[60:63], a[148:151], v[212:215], v[60:63]\n"
    "v_mfma_f32_16x16x32_bf16 v[60:63], a[152:155], v[216:219], v[60:63]\n"
    "v_mfma_f32_16x16x32_bf16 v[60:63], a[156:159], v[220:223], v[60:63]\n"
    "v_mfma_f32_16x16x32_bf16 v[64:67], a[128:131], v[224:227], 0\n"
    "v_mfma_f32_16x16x32_bf16 v[64:67], a[132:135], v[228:231], v[64:67]\n"
    "v_mfma_f32_16x16x32_bf16 v[64:67], a[136:139], v[232:235], v[64:67]\n"
    "v_mfma_f32_16x16x32_bf16 v[64:67], a[140:143], v[236:239], v[64:67]\n"
    "v_mfma_f32_16x16x32_bf16 v[68:71], a[144:147], v[224:227], 0\n"
    "v_mfma_f32_16x16x32_bf16 v[68:71], a[148:151], v[228:231], v[68:71]\n"
    "v_mfma_f32_16x16x32_bf16 v[68:71], a[152:155], v[232:235], v[68:71]\n"
    "v_mfma_f32_16x16x32_bf16 v[68:71], a[156:159], v[236:239], v[68:71]\n"
    "v_mfma_f32_16x16x32_bf16 v[72:75], a[128:131], v[240:243], 0\n"
    "v_mfma_f32_16x16x32_bf16 v[72:75], a[132:135], v[244:247], v[72:75]\n"
    "v_mfma_f32_16x16x32_bf16 v[72:75], a[136:139], v[248:251], v[72:75]\n"
    "v_mfma_f32_16x16x32_bf16 v[72:75], a[140:143], v[252:255], v[72:75]\n"
    "v_mfma_f32_16x16x32_bf16 v[76:79], a[144:147], v[240:243], 0\n"
    "v_mfma_f32_16x16x32_bf16 v[76:79], a[148:151], v[244:247], v[76:79]\n"
    "v_mfma_f32_16x16x32_bf16 v[76:79], a[152:155], v[248:251], v[76:79]\n"
    "v_mfma_f32_16x16x32_bf16 v[76:79], a[156:159], v[252:255], v[76:79]\n"
    "ds_read_b128 a[128:131], v2 offset:8192\n"
    "ds_read_b128 a[132:135], v2 offset:8704\n"
    "ds_read_b128 a[136:139], v2 offset:9216\n"
    "ds_read_b128 a[140:143], v2 offset:9728\n"
    "ds_read_b128 a[144:147], v2 offset:12288\n"
    "ds_read_b128 a[148:151], v2 offset:12800\n"
    "ds_read_b128 a[152:155], v2 offset:13312\n"
    "ds_read_b128 a[156:159], v2 offset:13824\n"
    "ds_read_b64_tr_b16 a[160:161], v3 offset:0\n"
    "ds_read_b64_tr_b16 a[162:163], v3 offset:4096\n"
    "ds_read_b64_tr_b16 a[164:165], v4 offset:0\n"
    "s_cmp_lg_u32 s72, 0\n"
    "s_cbranch_scc1 LHAVEM%=\n"
    "v_max3_f32 v11, v48, v49, v50\n"
    "v_max3_f32 v12, v56, v57, v58\n"
    "v_max3_f32 v13, v64, v65, v66\n"
    "v_max3_f32 v14, v72, v73, v74\n"
    "v_max3_f32 v11, v11, v51, v52\n"
    "v_max3_f32 v12, v12, v59, v60\n"
    "v_max3_f32 v13, v13, v67, v68\n"
    "v_max3_f32 v14, v14, v75, v76\n"
    "v_max3_f32 v11, v11, v53, v54\n"
    "v_max3_f32 v12, v12, v61, v62\n"
    "v_max3_f32 v13, v13, v69, v70\n"
    "v_max3_f32 v14, v14, v77, v78\n"
    "v_max_f32 v11, v11, v55\n"
    "v_max_f32 v12, v12, v63\n"
    "v_max_f32 v13, v13, v71\n"
    "v_max_f32 v14, v14, v79\n"
    "v_mov_b32 v112, v11\n"
    "v_mov_b32 v113, v12\n"
    "v_mov_b32 v114, v13\n"
    "v_mov_b32 v115, v14\n"
    "s_nop 0\n"
    "v_permlane16_swap_b32 v11, v112\n"
    "v_permlane16_swap_b32 v12, v113\n"
    "v_permlane16_swap_b32 v13, v114\n"
    "v_permlane16_swap_b32 v14, v115\n"
    "v_max_f32 v11, v11, v112\n"
    "v_max_f32 v12, v12, v113\n"
    "v_max_f32 v13, v13, v114\n"
    "v_max_f32 v14, v14, v115\n"
    "v_mov_b32 v112, v11\n"
    "v_mov_b32 v113, v12\n"
    "v_mov_b32 v114, v13\n"
    "v_mov_b32 v115, v14\n"
    "s_nop 0\n"
    "v_permlane32_swap_b32 v11, v112\n"
    "v_permlane32_swap_b32 v12, v113\n"
    "v_permlane32_swap_b32 v13, v114\n"
    "v_permlane32_swap_b32 v14, v115\n"
    "v_max_f32 v11, v11, v112\n"
    "v_max_f32 v12, v12, v113\n"
    "v_max_f32 v13, v13, v114\n"
    "v_max_f32 v14, v14, v115\n"
    "LHAVEM%=:\n"
    "v_sub_f32 v48, v48, v11\n"
    "v_sub_f32 v49, v49, v11\n"
    "v_sub_f32 v50, v50, v11\n"
    "v_sub_f32 v51, v51, v11\n"
    "v_sub_f32 v52, v52, v11\n"
    "v_sub_f32 v53, v53, v11\n"
    "v_sub_f32 v54, v54, v11\n"
    "v_sub_f32 v55, v55, v11\n"
    "v_sub_f32 v32, 0, v11\n"
    "v_sub_f32 v33, 0, v11\n"
    "v_sub_f32 v34, 0, v11\n"
    "v_sub_f32 v35, 0, v11\n"
    "v_sub_f32 v56, v56, v12\n"
    "v_sub_f32 v57, v57, v12\n"
    "v_sub_f32 v58, v58, v12\n"
    "v_sub_f32 v59, v59, v12\n"
    "v_sub_f32 v60, v60, v12\n"
    "v_sub_f32 v61, v61, v12\n"
    "v_sub_f32 v62, v62, v12\n"
    "v_sub_f32 v63, v63, v12\n"
    "v_sub_f32 v36, 0, v12\n"
    "v_sub_f32 v37, 0, v12\n"
    "v_sub_f32 v38, 0, v12\n"
    "v_sub_f32 v39, 0, v12\n"
    "v_sub_f32 v64, v64, v13\n"
    "v_sub_f32 v65, v65, v13\n"
    "v_sub_f32 v66, v66, v13\n"
    "v_sub_f32 v67, v67, v13\n"
    "v_sub_f32 v68, v68, v13\n"
    "v_sub_f32 v69, v69, v13\n"
    "v_sub_f32 v70, v70, v13\n"
    "v_sub_f32 v71, v71, v13\n"
    "v_sub_f32 v40, 0, v13\n"
    "v_sub_f32 v41, 0, v13\n"
    "v_sub_f32 v42, 0, v13\n"
    "v_sub_f32 v43, 0, v13\n"
    "v_sub_f32 v72, v72, v14\n"
    "v_sub_f32 v73, v73, v14\n"
    "v_sub_f32 v74, v74, v14\n"
    "v_sub_f32 v75, v75, v14\n"
    "v_sub_f32 v76, v76, v14\n"
    "v_sub_f32 v77, v77, v14\n"
    "v_sub_f32 v78, v78, v14\n"
    "v_sub_f32 v79, v79, v14\n"
    "v_sub_f32 v44, 0, v14\n"
    "v_sub_f32 v45, 0, v14\n"
    "v_sub_f32 v46, 0, v14\n"
    "v_sub_f32 v47, 0, v14\n"
    "v_exp_f32 v48, v48\n"
    "v_exp_f32 v49, v49\n"
    "v_exp_f32 v50, v50\n"
    "v_exp_f32 v51, v51\n"
    "v_exp_f32 v52, v52\n"
    "v_exp_f32 v53, v53\n"
    "v_exp_f32 v54, v54\n"
    "v_exp_f32 v55, v55\n"
    "v_exp_f32 v56, v56\n"
    "v_exp_f32 v57, v57\n"
    "v_exp_f32 v58, v58\n"
    "v_exp_f32 v59, v59\n"
    "v_exp_f32 v60, v60\n"
    "v_exp_f32 v61, v61\n"
    "v_exp_f32 v62, v62\n"
    "v_exp_f32 v63, v63\n"
    "v_exp_f32 v64, v64\n"
    "v_exp_f32 v65, v65\n"
    "v_exp_f32 v66, v66\n"
    "v_exp_f32 v67, v67\n"
    "v_exp_f32 v68, v68\n"
    "v_exp_f32 v69, v69\n"
    "v_exp_f32 v70, v70\n"
    "v_exp_f32 v71, v71\n"
    "v_exp_f32 v72, v72\n"
    "v_exp_f32 v73, v73\n"
    "v_cvt_pk_bf16_f32 v80, v48, v49\n"
    "v_cvt_pk_bf16_f32 v81, v50, v51\n"
    "v_cvt_pk_bf16_f32 v82, v52, v53\n"
    "v_cvt_pk_bf16_f32 v83, v54, v55\n"
    "v_cvt_pk_bf16_f32 v84, v56, v57\n"
    "v_cvt_pk_bf16_f32 v85, v58, v59\n"
    "v_cvt_pk_bf16_f32 v86, v60, v61\n"
    "v_cvt_pk_bf16_f32 v87, v62, v63\n"
    "v_cvt_pk_bf16_f32 v88, v64, v65\n"
    "v_cvt_pk_bf16_f32 v89, v66, v67\n"
    "v_cvt_pk_bf16_f32 v90, v68, v69\n"
    "v_cvt_pk_bf16_f32 v91, v70, v71\n"
    "LLOOP%=:\n"
    "s_waitcnt lgkmcnt(3)\n"
    "v_mfma_f32_16x16x32_bf16 v[48:51], a[128:131], v[192:195], v[32:35]\n"
    "ds_read_b64_tr_b16 a[166:167], v4 offset:4096\n"
    "v_cvt_pk_bf16_f32 v92, v72, v73\n"
    "v_mfma_f32_16x16x32_bf16 v[48:51], a[132:135], v[196:199], v[48:51]\n"
    "v_exp_f32 v74, v74\n"
    "v_mfma_f32_16x16x32_bf16 v[48:51], a[136:139], v[200:203], v[48:51]\n"
    "ds_read_b64_tr_b16 a[168:169], v3 offset:512\n"
    "s_add_u32 s68, s68, 16384\n"
    "v_mfma_f32_16x16x32_bf16 v[48:51], a[140:143], v[204:207], v[48:51]\n"
    "v_exp_f32 v75, v75\n"
    "v_mfma_f32_16x16x32_bf16 v[52:55], a[144:147], v[192:195], v[32:35]\n"
    "ds_read_b64_tr_b16 a[170:171], v3 offset:4608\n"
    "v_cvt_pk_bf16_f32 v93, v74, v75\n"
    "v_mfma_f32_16x16x32_bf16 v[52:55], a[148:151], v[196:199], v[52:55]\n"
    "v_exp_f32 v76, v76\n"
    "v_mfma_f32_16x16x32_bf16 v[52:55], a[152:155], v[200:203], v[52:55]\n"
    "ds_read_b64_tr_b16 a[172:173], v4 offset:512\n"
    "s_and_b32 s69, s68, 0x7ffff\n"
    "v_mfma_f32_16x16x32_bf16 v[52:55], a[156:159], v[204:207], v[52:55]\n"
    "v_exp_f32 v77, v77\n"
    "v_mfma_f32_16x16x32_bf16 v[56:59], a[128:131], v[208:211], v[36:39]\n"
    "ds_read_b64_tr_b16 a[174:175], v4 offset:4608\n"
    "v_cvt_pk_bf16_f32 v94, v76, v77\n"
    "v_mfma_f32_16x16x32_bf16 v[56:59], a[132:135], v[212:215], v[56:59]\n"
    "v_exp_f32 v78, v78\n"
    "v_mfma_f32_16x16x32_bf16 v[56:59], a[136:139], v[216:219], v[56:59]\n"
    "ds_read_b64_tr_b16 a[176:177], v3 offset:1024\n"
    "v_mfma_f32_16x16x32_bf16 v[56:59], a[140:143], v[220:223], v[56:59]\n"
    "v_exp_f32 v79, v79\n"
    "v_mfma_f32_16x16x32_bf16 v[60:63], a[144:147], v[208:211], v[36:39]\n"
    "ds_read_b64_tr_b16 a[178:179], v3 offset:5120\n"
    "v_cvt_pk_bf16_f32 v95, v78, v79\n"
    "v_mfma_f32_16x16x32_bf16 v[60:63], a[148:151], v[212:215], v[60:63]\n"
    "v_exp_f32 v48, v48\n"
    "v_mfma_f32_16x16x32_bf16 v[60:63], a[152:155], v[216:219], v[60:63]\n"
    "ds_read_b64_tr_b16 a[180:181], v4 offset:1024\n"
    "v_mfma_f32_16x16x32_bf16 v[60:63], a[156:159], v[220:223], v[60:63]\n"
    "v_exp_f32 v49, v49\n"
    "v_mfma_f32_16x16x32_bf16 v[64:67], a[128:131], v[224:227], v[40:43]\n"
    "ds_read_b64_tr_b16 a[182:183], v4 offset:5120\n"
    "v_cvt_pk_bf16_f32 v96, v48, v49\n"
    "v_mfma_f32_16x16x32_bf16 v[64:67], a[132:135], v[228:231], v[64:67]\n"
    "v_exp_f32 v50, v50\n"
    "v_mfma_f32_16x16x32_bf16 v[64:67], a[136:139], v[232:235], v[64:67]\n"
    "ds_read_b64_tr_b16 a[184:185], v3 offset:1536\n"
    "v_mfma_f32_16x16x32_bf16 v[64:67], a[140:143], v[236:239], v[64:67]\n"
    "v_exp_f32 v51, v51\n"
    "v_mfma_f32_16x16x32_bf16 v[68:71], a[144:147], v[224:227], v[40:43]\n"
    "ds_read_b64_tr_b16 a[186:187], v3 offset:5632\n"
    "v_cvt_pk_bf16_f32 v97, v50, v51\n"
    "v_mfma_f32_16x16x32_bf16 v[68:71], a[148:151], v[228:231], v[68:71]\n"
    "v_exp_f32 v52, v52\n"
    "v_mfma_f32_16x16x32_bf16 v[68:71], a[152:155], v[232:235], v[68:71]\n"
    "ds_read_b64_tr_b16 a[188:189], v4 offset:1536\n"
    "v_mfma_f32_16x16x32_bf16 v[68:71], a[156:159], v[236:239], v[68:71]\n"
    "v_exp_f32 v53, v53\n"
    "v_mfma_f32_16x16x32_bf16 v[72:75], a[128:131], v[240:243], v[44:47]\n"
    "ds_read_b64_tr_b16 a[190:191], v4 offset:5632\n"
    "v_cvt_pk_bf16_f32 v98, v52, v53\n"
    "v_mfma_f32_16x16x32_bf16 v[72:75], a[132:135], v[244:247], v[72:75]\n"
    "v_exp_f32 v54, v54\n"
    "v_mfma_f32_16x16x32_bf16 v[72:75], a[136:139], v[248:251], v[72:75]\n"
    "ds_read_b128 a[128:131], v2 offset:16384\n"
    "s_add_u32 m0, s70, 49152\n"
    "v_mfma_f32_16x16x32_bf16 v[72:75], a[140:143], v[252:255], v[72:75]\n"
    "v_exp_f32 v55, v55\n"
    "v_mfma_f32_16x16x32_bf16 v[76:79], a[144:147], v[240:243], v[44:47]\n"
    "buffer_load_dwordx4 v5, s[40:43], s69 offen offset:0 lds\n"
    "v_cvt_pk_bf16_f32 v99, v54, v55\n"
    "v_mfma_f32_16x16x32_bf16 v[76:79], a[148:151], v[244:247], v[76:79]\n"
    "v_exp_f32 v56, v56\n"
    "v_mfma_f32_16x16x32_bf16 v[76:79], a[152:155], v[248:251], v[76:79]\n"
    "ds_read_b128 a[132:135], v2 offset:16896\n"
    "v_mfma_f32_16x16x32_bf16 v[76:79], a[156:159], v[252:255], v[76:79]\n"
    "v_exp_f32 v57, v57\n"
    "s_waitcnt lgkmcnt(2)\n"
    "v_mfma_f32_16x16x32_bf16 a[0:3], a[160:163], v[80:83], a[0:3]\n"
    "buffer_load_dwordx4 v6, s[40:43], s69 offen offset:1024 lds\n"
    "v_cvt_pk_bf16_f32 v100, v56, v57\n"
    "v_mfma_f32_16x16x32_bf16 a[4:7], a[164:167], v[80:83], a[4:7]\n"
    "v_exp_f32 v58, v58\n"
    "v_mfma_f32_16x16x32_bf16 a[8:11], a[168:171], v[80:83], a[8:11]\n"
    "ds_read_b128 a[136:139], v2 offset:17408\n"
    "v_mfma_f32_16x16x32_bf16 a[12:15], a[172:175], v[80:83], a[12:15]\n"
    "v_exp_f32 v59, v59\n"
    "v_mfma_f32_16x16x32_bf16 a[16:19], a[176:179], v[80:83], a[16:19]\n"
    "buffer_load_dwordx4 v7, s[40:43], s69 offen offset:2048 lds\n"
    "v_cvt_pk_bf16_f32 v101, v58, v59\n"
    "v_mfma_f32_16x16x32_bf16 a[20:23], a[180:183], v[80:83], a[20:23]\n"
    "v_exp_f32 v60, v60\n"
    "v_mfma_f32_16x16x32_bf16 a[24:27], a[184:187], v[80:83], a[24:27]\n"
    "ds_read_b128 a[140:143], v2 offset:17920\n"
    "v_mfma_f32_16x16x32_bf16 a[28:31], a[188:191], v[80:83], a[28:31]\n"
    "v_exp_f32 v61, v61\n"
    "v_mfma_f32_16x16x32_bf16 a[208:211], a[232:235], v[80:83], a[208:211]\n"
    "v_mfma_f32_16x16x32_bf16 a[32:35], a[160:163], v[84:87], a[32:35]\n"
    "buffer_load_dwordx4 v8, s[40:43], s69 offen offset:3072 lds\n"
    "v_cvt_pk_bf16_f32 v102, v60, v61\n"
    "v_mfma_f32_16x16x32_bf16 a[36:39], a[164:167], v[84:87], a[36:39]\n"
    "v_exp_f32 v62, v62\n"
    "v_mfma_f32_16x16x32_bf16 a[40:43], a[168:171], v[84:87], a[40:43]\n"
    "ds_read_b128 a[144:147], v2 offset:20480\n"
    "s_add_u32 m0, s71, 49152\n"
    "v_mfma_f32_16x16x32_bf16 a[44:47], a[172:175], v[84:87], a[44:47]\n"
    "v_exp_f32 v63, v63\n"
    "v_mfma_f32_16x16x32_bf16 a[48:51], a[176:179], v[84:87], a[48:51]\n"
    "buffer_load_dwordx4 v9, s[44:47], s69 offen offset:0 lds\n"
    "v_cvt_pk_bf16_f32 v103, v62, v63\n"
    "v_mfma_f32_16x16x32_bf16 a[52:55], a[180:183], v[84:87], a[52:55]\n"
    "v_exp_f32 v64, v64\n"
    "v_mfma_f32_16x16x32_bf16 a[56:59], a[184:187], v[84:87], a[56:59]\n"
    "ds_read_b128 a[148:151], v2 offset:20992\n"
    "v_mfma_f32_16x16x32_bf16 a[60:63], a[188:191], v[84:87], a[60:63]\n"
    "v_exp_f32 v65, v65\n"
    "v_mfma_f32_16x16x32_bf16 a[212:215], a[232:235], v[84:87], a[212:215]\n"
    "v_mfma_f32_16x16x32_bf16 a[64:67], a[160:163], v[88:91], a[64:67]\n"
    "buffer_load_dwordx4 v10, s[44:47], s69 offen offset:1024 lds\n"
    "v_cvt_pk_bf16_f32 v104, v64, v65\n"
    "v_mfma_f32_16x16x32_bf16 a[68:71], a[164:167], v[88:91], a[68:71]\n"
    "v_exp_f32 v66, v66\n"
    "v_mfma_f32_16x16x32_bf16 a[72:75], a[168:171], v[88:91], a[72:75]\n"
    "ds_read_b128 a[152:155], v2 offset:21504\n"
    "v_mfma_f32_16x16x32_bf16 a[76:79], a[172:175], v[88:91], a[76:79]\n"
    "v_exp_f32 v67, v67\n"
    "v_mfma_f32_16x16x32_bf16 a[80:83], a[176:179], v[88:91], a[80:83]\n"
    "buffer_load_dwordx4 v9, s[44:47], s69 offen offset:2048 lds\n"
    "v_cvt_pk_bf16_f32 v105, v66, v67\n"
    "v_mfma_f32_16x16x32_bf16 a[84:87], a[180:183], v[88:91], a[84:87]\n"
    "v_exp_f32 v68, v68\n"
    "v_mfma_f32_16x16x32_bf16 a[88:91], a[184:187], v[88:91], a[88:91]\n"
    "ds_read_b128 a[156:159], v2 offset:22016\n"
    "v_mfma_f32_16x16x32_bf16 a[92:95], a[188:191], v[88:91], a[92:95]\n"
    "v_exp_f32 v69, v69\n"
    "v_mfma_f32_16x16x32_bf16 a[216:219], a[232:235], v[88:91], a[216:219]\n"
    "v_mfma_f32_16x16x32_bf16 a[96:99], a[160:163], v[92:95], a[96:99]\n"
    "buffer_load_dwordx4 v10, s[44:47], s69 offen offset:3072 lds\n"
    "v_cvt_pk_bf16_f32 v106, v68, v69\n"
    "v_mfma_f32_16x16x32_bf16 a[100:103], a[164:167], v[92:95], a[100:103]\n"
    "v_exp_f32 v70, v70\n"
    "v_mfma_f32_16x16x32_bf16 a[104:107], a[168:171], v[92:95], a[104:107]\n"
    "ds_read_b64_tr_b16 a[160:161], v3 offset:8192\n"
    "v_mfma_f32_16x16x32_bf16 a[108:111], a[172:175], v[92:95], a[108:111]\n"
    "v_exp_f32 v71, v71\n"
    "v_mfma_f32_16x16x32_bf16 a[112:115], a[176:179], v[92:95], a[112:115]\n"
    "ds_read_b64_tr_b16 a[162:163], v3 offset:12288\n"
    "v_cvt_pk_bf16_f32 v107, v70, v71\n"
    "v_mfma_f32_16x16x32_bf16 a[116:119], a[180:183], v[92:95], a[116:119]\n"
    "v_exp_f32 v72, v72\n"
    "v_mfma_f32_16x16x32_bf16 a[120:123], a[184:187], v[92:95], a[120:123]\n"
    "ds_read_b64_tr_b16 a[164:165], v4 offset:8192\n"
    "v_mfma_f32_16x16x32_bf16 a[124:127], a[188:191], v[92:95], a[124:127]\n"
    "v_exp_f32 v73, v73\n"
    "v_mfma_f32_16x16x32_bf16 a[220:223], a[232:235], v[92:95], a[220:223]\n"
    "s_waitcnt lgkmcnt(3)\n"
    "v_mfma_f32_16x16x32_bf16 v[48:51], a[128:131], v[192:195], v[32:35]\n"
    "ds_read_b64_tr_b16 a[166:167], v4 offset:12288\n"
    "v_cvt_pk_bf16_f32 v108, v72, v73\n"
    "v_mfma_f32_16x16x32_bf16 v[48:51], a[132:135], v[196:199], v[48:51]\n"
    "v_exp_f32 v74, v74\n"
    "v_mfma_f32_16x16x32_bf16 v[48:51], a[136:139], v[200:203], v[48:51]\n"
    "ds_read_b64_tr_b16 a[168:169], v3 offset:8704\n"
    "v_mfma_f32_16x16x32_bf16 v[48:51], a[140:143], v[204:207], v[48:51]\n"
    "v_exp_f32 v75, v75\n"
    "v_mfma_f32_16x16x32_bf16 v[52:55], a[144:147], v[192:195], v[32:35]\n"
    "ds_read_b64_tr_b16 a[170:171], v3 offset:12800\n"
    "v_cvt_pk_bf16_f32 v109, v74, v75\n"
    "v_mfma_f32_16x16x32_bf16 v[52:55], a[148:151], v[196:199], v[52:55]\n"
    "v_exp_f32 v76, v76\n"
    "v_mfma_f32_16x16x32_bf16 v[52:55], a[152:155], v[200:203], v[52:55]\n"
    "ds_read_b64_tr_b16 a[172:173], v4 offset:8704\n"
    "v_mfma_f32_16x16x32_bf16 v[52:55], a[156:159], v[204:207], v[52:55]\n"
    "v_exp_f32 v77, v77\n"
    "v_mfma_f32_16x16x32_bf16 v[56:59], a[128:131], v[208:211], v[36:39]\n"
    "ds_read_b64_tr_b16 a[174:175], v4 offset:12800\n"
    "v_cvt_pk_bf16_f32 v110, v76, v77\n"
    "v_mfma_f32_16x16x32_bf16 v[56:59], a[132:135], v[212:215], v[56:59]\n"
    "v_exp_f32 v78, v78\n"
    "v_mfma_f32_16x16x32_bf16 v[56:59], a[136:139], v[216:219], v[56:59]\n"
    "ds_read_b64_tr_b16 a[176:177], v3 offset:9216\n"
    "v_mfma_f32_16x16x32_bf16 v[56:59], a[140:143], v[220:223], v[56:59]\n"
    "v_exp_f32 v79, v79\n"
    "v_mfma_f32_16x16x32_bf16 v[60:63], a[144:147], v[208:211], v[36:39]\n"
    "ds_read_b64_tr_b16 a[178:179], v3 offset:13312\n"
    "v_cvt_pk_bf16_f32 v111, v78, v79\n"
    "v_mfma_f32_16x16x32_bf16 v[60:63], a[148:151], v[212:215], v[60:63]\n"
    "v_exp_f32 v48, v48\n"
    "v_mfma_f32_16x16x32_bf16 v[60:63], a[152:155], v[216:219], v[60:63]\n"
    "ds_read_b64_tr_b16 a[180:181], v4 offset:9216\n"
    "v_mfma_f32_16x16x32_bf16 v[60:63], a[156:159], v[220:223], v[60:63]\n"
    "v_exp_f32 v49, v49\n"
    "v_mfma_f32_16x16x32_bf16 v[64:67], a[128:131], v[224:227], v[40:43]\n"
    "ds_read_b64_tr_b16 a[182:183], v4 offset:13312\n"
    "v_cvt_pk_bf16_f32 v80, v48, v49\n"
    "v_mfma_f32_16x16x32_bf16 v[64:67], a[132:135], v[228:231], v[64:67]\n"
    "v_exp_f32 v50, v50\n"
    "v_mfma_f32_16x16x32_bf16 v[64:67], a[136:139], v[232:235], v[64:67]\n"
    "ds_read_b64_tr_b16 a[184:185], v3 offset:9728\n"
    "v_mfma_f32_16x16x32_bf16 v[64:67], a[140:143], v[236:239], v[64:67]\n"
    "v_exp_f32 v51, v51\n"
    "v_mfma_f32_16x16x32_bf16 v[68:71], a[144:147], v[224:227], v[40:43]\n"
    "ds_read_b64_tr_b16 a[186:187], v3 offset:13824\n"
    "v_cvt_pk_bf16_f32 v81, v50, v51\n"
    "v_mfma_f32_16x16x32_bf16 v[68:71], a[148:151], v[228:231], v[68:71]\n"
    "v_exp_f32 v52, v52\n"
    "v_mfma_f32_16x16x32_bf16 v[68:71], a[152:155], v[232:235], v[68:71]\n"
    "ds_read_b64_tr_b16 a[188:189], v4 offset:9728\n"
    "v_mfma_f32_16x16x32_bf16 v[68:71], a[156:159], v[236:239], v[68:71]\n"
    "v_exp_f32 v53, v53\n"
    "v_mfma_f32_16x16x32_bf16 v[72:75], a[128:131], v[240:243], v[44:47]\n"
    "ds_read_b64_tr_b16 a[190:191], v4 offset:13824\n"
    "v_cvt_pk_bf16_f32 v82, v52, v53\n"
    "v_mfma_f32_16x16x32_bf16 v[72:75], a[132:135], v[244:247], v[72:75]\n"
    "v_exp_f32 v54, v54\n"
    "v_mfma_f32_16x16x32_bf16 v[72:75], a[136:139], v[248:251], v[72:75]\n"
    "ds_read_b128 a[128:131], v2 offset:24576\n"
    "v_mfma_f32_16x16x32_bf16 v[72:75], a[140:143], v[252:255], v[72:75]\n"
    "v_exp_f32 v55, v55\n"
    "v_mfma_f32_16x16x32_bf16 v[76:79], a[144:147], v[240:243], v[44:47]\n"
    "v_cvt_pk_bf16_f32 v83, v54, v55\n"
    "v_mfma_f32_16x16x32_bf16 v[76:79], a[148:151], v[244:247], v[76:79]\n"
    "v_exp_f32 v56, v56\n"
    "v_mfma_f32_16x16x32_bf16 v[76:79], a[152:155], v[248:251], v[76:79]\n"
    "ds_read_b128 a[132:135], v2 offset:25088\n"
    "v_mfma_f32_16x16x32_bf16 v[76:79], a[156:159], v[252:255], v[76:79]\n"
    "v_exp_f32 v57, v57\n"
    "s_cmp_lg_u32 s67, 7\n"
    "s_cbranch_scc1 LNOGU%=\n"
    "v_lshlrev_b32 v116, 4, v1\n"
    "v_and_b32 v117, 31, v1\n"
    "v_lshlrev_b32 v118, 7, v117\n"
    "v_lshlrev_b32 v117, 4, v117\n"
    "v_lshrrev_b32 v119, 5, v1\n"
    "v_lshl_add_u32 v118, v119, 4, v118\n"
    "v_lshl_add_u32 v117, v119, 14, v117\n"
    "v_add_u32 v118, s70, v118\n"
    "v_add_u32 v118, 131072, v118\n"
    "ds_read_b128 v[144:147], v118 offset:0\n"
    "ds_read_b128 v[148:151], v118 offset:32\n"
    "ds_read_b128 v[152:155], v118 offset:64\n"
    "ds_read_b128 v[156:159], v118 offset:96\n"
    "s_mul_i32 s73, s66, 544768\n"
    "s_lshl_b32 s74, s65, 12\n"
    "s_add_u32 s73, s73, s74\n"
    "s_lshl_b32 s74, s64, 10\n"
    "s_add_u32 s73, s73, s74\n"
    "s_add_u32 s74, s73, 136192\n"
    "s_add_u32 s75, s73, 272384\n"
    "s_add_u32 s76, s73, 408576\n"
    "buffer_store_dwordx4 a[192:195], v116, s[56:59], s73 offen sc1\n"
    "buffer_store_dwordx4 a[196:199], v116, s[56:59], s74 offen sc1\n"
    "buffer_store_dwordx4 a[200:203], v116, s[56:59], s75 offen sc1\n"
    "buffer_store_dwordx4 a[204:207], v116, s[56:59], s76 offen sc1\n"
    "s_lshr_b32 s73, s31, 5\n"
    "s_lshl_b32 s73, s73, 17\n"
    "s_and_b32 s74, s31, 1\n"
    "s_lshl_b32 s74, s74, 13\n"
    "s_add_u32 s73, s73, s74\n"
    "s_bfe_u32 s74, s31, 0x40001\n"
    "s_lshl_b32 s74, s74, 9\n"
    "s_add_u32 s73, s73, s74\n"
    "s_add_u32 s73, s73, 2097152\n"
    "s_waitcnt lgkmcnt(0)\n"
    "s_cmp_gt_u32 %7, 175\n"
    "s_cbranch_scc1 LNOGU%=\n"
    "buffer_store_dwordx4 v[144:147], v117, s[60:63], s73 offen sc1\n"
    "s_add_u32 s73, s73, 32768\n"
    "buffer_store_dwordx4 v[148:151], v117, s[60:63], s73 offen sc1\n"
    "s_add_u32 s73, s73, 32768\n"
    "buffer_store_dwordx4 v[152:155], v117, s[60:63], s73 offen sc1\n"
    "s_add_u32 s73, s73, 32768\n"
    "buffer_store_dwordx4 v[156:159], v117, s[60:63], s73 offen sc1\n"
    "s_add_u32 s73, s73, 32768\n"
    "s_lshl_b32 s75, %7, 11\n"
    "s_lshl_b32 s76, s64, 9\n"
    "s_add_u32 s75, s75, s76\n"
    "s_add_u32 s75, s75, 1048576\n"
    "v_mov_b32 v176, -1\n"
    "v_mov_b32 v177, -1\n"
    "v_lshlrev_b32 v117, 3, v1\n"
    "buffer_store_dwordx2 v[176:177], v117, s[60:63], s75 offen sc1\n"
    "LNOGU%=:\n"
    "s_waitcnt lgkmcnt(2)\n"
    "v_mfma_f32_16x16x32_bf16 a[0:3], a[160:163], v[96:99], a[0:3]\n"
    "v_cvt_pk_bf16_f32 v84, v56, v57\n"
    "v_mfma_f32_16x16x32_bf16 a[4:7], a[164:167], v[96:99], a[4:7]\n"
    "v_exp_f32 v58, v58\n"
    "v_mfma_f32_16x16x32_bf16 a[8:11], a[168:171], v[96:99], a[8:11]\n"
    "ds_read_b128 a[136:139], v2 offset:25600\n"
    "v_mfma_f32_16x16x32_bf16 a[12:15], a[172:175], v[96:99], a[12:15]\n"
    "v_exp_f32 v59, v59\n"
    "v_mfma_f32_16x16x32_bf16 a[16:19], a[176:179], v[96:99], a[16:19]\n"
    "v_cvt_pk_bf16_f32 v85, v58, v59\n"
    "v_mfma_f32_16x16x32_bf16 a[20:23], a[180:183], v[96:99], a[20:23]\n"
    "v_exp_f32 v60, v60\n"
    "v_mfma_f32_16x16x32_bf16 a[24:27], a[184:187], v[96:99], a[24:27]\n"
    "ds_read_b128 a[140:143], v2 offset:26112\n"
    "v_mfma_f32_16x16x32_bf16 a[28:31], a[188:191], v[96:99], a[28:31]\n"
    "v_exp_f32 v61, v61\n"
    "v_mfma_f32_16x16x32_bf16 a[208:211], a[232:235], v[96:99], a[208:211]\n"
    "v_mfma_f32_16x16x32_bf16 a[32:35], a[160:163], v[100:103], a[32:35]\n"
    "v_cvt_pk_bf16_f32 v86, v60, v61\n"
    "v_mfma_f32_16x16x32_bf16 a[36:39], a[164:167], v[100:103], a[36:39]\n"
    "v_exp_f32 v62, v62\n"
    "v_mfma_f32_16x16x32_bf16 a[40:43], a[168:171], v[100:103], a[40:43]\n"
    "ds_read_b128 a[144:147], v2 offset:28672\n"
    "v_mfma_f32_16x16x32_bf16 a[44:47], a[172:175], v[100:103], a[44:47]\n"
    "v_exp_f32 v63, v63\n"
    "v_mfma_f32_16x16x32_bf16 a[48:51], a[176:179], v[100:103], a[48:51]\n"
    "v_cvt_pk_bf16_f32 v87, v62, v63\n"
    "v_mfma_f32_16x16x32_bf16 a[52:55], a[180:183], v[100:103], a[52:55]\n"
    "v_exp_f32 v64, v64\n"
    "v_mfma_f32_16x16x32_bf16 a[56:59], a[184:187], v[100:103], a[56:59]\n"
    "ds_read_b128 a[148:151], v2 offset:29184\n"
    "v_mfma_f32_16x16x32_bf16 a[60:63], a[188:191], v[100:103], a[60:63]\n"
    "v_exp_f32 v65, v65\n"
    "v_mfma_f32_16x16x32_bf16 a[212:215], a[232:235], v[100:103], a[212:215]\n"
    "v_mfma_f32_16x16x32_bf16 a[64:67], a[160:163], v[104:107], a[64:67]\n"
    "v_cvt_pk_bf16_f32 v88, v64, v65\n"
    "v_mfma_f32_16x16x32_bf16 a[68:71], a[164:167], v[104:107], a[68:71]\n"
    "v_exp_f32 v66, v66\n"
    "v_mfma_f32_16x16x32_bf16 a[72:75], a[168:171], v[104:107], a[72:75]\n"
    "ds_read_b128 a[152:155], v2 offset:29696\n"
    "v_mfma_f32_16x16x32_bf16 a[76:79], a[172:175], v[104:107], a[76:79]\n"
    "v_exp_f32 v67, v67\n"
    "v_mfma_f32_16x16x32_bf16 a[80:83], a[176:179], v[104:107], a[80:83]\n"
    "v_cvt_pk_bf16_f32 v89, v66, v67\n"
    "v_mfma_f32_16x16x32_bf16 a[84:87], a[180:183], v[104:107], a[84:87]\n"
    "v_exp_f32 v68, v68\n"
    "v_mfma_f32_16x16x32_bf16 a[88:91], a[184:187], v[104:107], a[88:91]\n"
    "ds_read_b128 a[156:159], v2 offset:30208\n"
    "v_mfma_f32_16x16x32_bf16 a[92:95], a[188:191], v[104:107], a[92:95]\n"
    "v_exp_f32 v69, v69\n"
    "v_mfma_f32_16x16x32_bf16 a[216:219], a[232:235], v[104:107], a[216:219]\n"
    "v_mfma_f32_16x16x32_bf16 a[96:99], a[160:163], v[108:111], a[96:99]\n"
    "v_cvt_pk_bf16_f32 v90, v68, v69\n"
    "s_waitcnt vmcnt(12)\n"
    "s_barrier\n"
    "v_mfma_f32_16x16x32_bf16 a[100:103], a[164:167], v[108:111], a[100:103]\n"
    "v_exp_f32 v70, v70\n"
    "v_mfma_f32_16x16x32_bf16 a[104:107], a[168:171], v[108:111], a[104:107]\n"
    "ds_read_b64_tr_b16 a[160:161], v3 offset:16384\n"
    "v_mfma_f32_16x16x32_bf16 a[108:111], a[172:175], v[108:111], a[108:111]\n"
    "v_exp_f32 v71, v71\n"
    "v_mfma_f32_16x16x32_bf16 a[112:115], a[176:179], v[108:111], a[112:115]\n"
    "ds_read_b64_tr_b16 a[162:163], v3 offset:20480\n"
    "v_cvt_pk_bf16_f32 v91, v70, v71\n"
    "v_mfma_f32_16x16x32_bf16 a[116:119], a[180:183], v[108:111], a[116:119]\n"
    "v_exp_f32 v72, v72\n"
    "v_mfma_f32_16x16x32_bf16 a[120:123], a[184:187], v[108:111], a[120:123]\n"
    "ds_read_b64_tr_b16 a[164:165], v4 offset:16384\n"
    "v_mfma_f32_16x16x32_bf16 a[124:127], a[188:191], v[108:111], a[124:127]\n"
    "v_exp_f32 v73, v73\n"
    "v_mfma_f32_16x16x32_bf16 a[220:223], a[232:235], v[108:111], a[220:223]\n"
    "s_waitcnt lgkmcnt(3)\n"
    "v_mfma_f32_16x16x32_bf16 v[48:51], a[128:131], v[192:195], v[32:35]\n"
    "ds_read_b64_tr_b16 a[166:167], v4 offset:20480\n"
    "v_cvt_pk_bf16_f32 v92, v72, v73\n"
    "v_mfma_f32_16x16x32_bf16 v[48:51], a[132:135], v[196:199], v[48:51]\n"
    "v_exp_f32 v74, v74\n"
    "v_mfma_f32_16x16x32_bf16 v[48:51], a[136:139], v[200:203], v[48:51]\n"
    "ds_read_b64_tr_b16 a[168:169], v3 offset:16896\n"
    "s_add_u32 s68, s68, 16384\n"
    "v_mfma_f32_16x16x32_bf16 v[48:51], a[140:143], v[204:207], v[48:51]\n"
    "v_exp_f32 v75, v75\n"
    "v_mfma_f32_16x16x32_bf16 v[52:55], a[144:147], v[192:195], v[32:35]\n"
    "ds_read_b64_tr_b16 a[170:171], v3 offset:20992\n"
    "v_cvt_pk_bf16_f32 v93, v74, v75\n"
    "v_mfma_f32_16x16x32_bf16 v[52:55], a[148:151], v[196:199], v[52:55]\n"
    "v_exp_f32 v76, v76\n"
    "v_mfma_f32_16x16x32_bf16 v[52:55], a[152:155], v[200:203], v[52:55]\n"
    "ds_read_b64_tr_b16 a[172:173], v4 offset:16896\n"
    "s_and_b32 s69, s68, 0x7ffff\n"
    "v_mfma_f32_16x16x32_bf16 v[52:55], a[156:159], v[204:207], v[52:55]\n"
    "v_exp_f32 v77, v77\n"
    "v_mfma_f32_16x16x32_bf16 v[56:59], a[128:131], v[208:211], v[36:39]\n"
    "ds_read_b64_tr_b16 a[174:175], v4 offset:20992\n"
    "v_cvt_pk_bf16_f32 v94, v76, v77\n"
    "v_mfma_f32_16x16x32_bf16 v[56:59], a[132:135], v[212:215], v[56:59]\n"
    "v_exp_f32 v78, v78\n"
    "v_mfma_f32_16x16x32_bf16 v[56:59], a[136:139], v[216:219], v[56:59]\n"
    "ds_read_b64_tr_b16 a[176:177], v3 offset:17408\n"
    "v_mfma_f32_16x16x32_bf16 v[56:59], a[140:143], v[220:223], v[56:59]\n"
    "v_exp_f32 v79, v79\n"
    "v_mfma_f32_16x16x32_bf16 v[60:63], a[144:147], v[208:211], v[36:39]\n"
    "ds_read_b64_tr_b16 a[178:179], v3 offset:21504\n"
    "v_cvt_pk_bf16_f32 v95, v78, v79\n"
    "v_mfma_f32_16x16x32_bf16 v[60:63], a[148:151], v[212:215], v[60:63]\n"
    "v_exp_f32 v48, v48\n"
    "v_mfma_f32_16x16x32_bf16 v[60:63], a[152:155], v[216:219], v[60:63]\n"
    "ds_read_b64_tr_b16 a[180:181], v4 offset:17408\n"
    "v_mfma_f32_16x16x32_bf16 v[60:63], a[156:159], v[220:223], v[60:63]\n"
    "v_exp_f32 v49, v49\n"
    "v_mfma_f32_16x16x32_bf16 v[64:67], a[128:131], v[224:227], v[40:43]\n"
    "ds_read_b64_tr_b16 a[182:183], v4 offset:21504\n"
    "v_cvt_pk_bf16_f32 v96, v48, v49\n"
    "v_mfma_f32_16x16x32_bf16 v[64:67], a[132:135], v[228:231], v[64:67]\n"
    "v_exp_f32 v50, v50\n"
    "v_mfma_f32_16x16x32_bf16 v[64:67], a[136:139], v[232:235], v[64:67]\n"
    "ds_read_b64_tr_b16 a[184:185], v3 offset:17920\n"
    "v_mfma_f32_16x16x32_bf16 v[64:67], a[140:143], v[236:239], v[64:67]\n"
    "v_exp_f32 v51, v51\n"
    "v_mfma_f32_16x16x32_bf16 v[68:71], a[144:147], v[224:227], v[40:43]\n"
    "ds_read_b64_tr_b16 a[186:187], v3 offset:22016\n"
    "v_cvt_pk_bf16_f32 v97, v50, v51\n"
    "v_mfma_f32_16x16x32_bf16 v[68:71], a[148:151], v[228:231], v[68:71]\n"
    "v_exp_f32 v52, v52\n"
    "v_mfma_f32_16x16x32_bf16 v[68:71], a[152:155], v[232:235], v[68:71]\n"
    "ds_read_b64_tr_b16 a[188:189], v4 offset:17920\n"
    "v_mfma_f32_16x16x32_bf16 v[68:71], a[156:159], v[236:239], v[68:71]\n"
    "v_exp_f32 v53, v53\n"
    "v_mfma_f32_16x16x32_bf16 v[72:75], a[128:131], v[240:243], v[44:47]\n"
    "ds_read_b64_tr_b16 a[190:191], v4 offset:22016\n"
    "v_cvt_pk_bf16_f32 v98, v52, v53\n"
    "v_mfma_f32_16x16x32_bf16 v[72:75], a[132:135], v[244:247], v[72:75]\n"
    "v_exp_f32 v54, v54\n"
    "v_mfma_f32_16x16x32_bf16 v[72:75], a[136:139], v[248:251], v[72:75]\n"
    "ds_read_b128 a[128:131], v2 offset:32768\n"
    "s_add_u32 m0, s70, 0\n"
    "v_mfma_f32_16x16x32_bf16 v[72:75], a[140:143], v[252:255], v[72:75]\n"
    "v_exp_f32 v55, v55\n"
    "v_mfma_f32_16x16x32_bf16 v[76:79], a[144:147], v[240:243], v[44:47]\n"
    "buffer_load_dwordx4 v5, s[40:43], s69 offen offset:0 lds\n"
    "v_cvt_pk_bf16_f32 v99, v54, v55\n"
    "v_mfma_f32_16x16x32_bf16 v[76:79], a[148:151], v[244:247], v[76:79]\n"
    "v_exp_f32 v56, v56\n"
    "v_mfma_f32_16x16x32_bf16 v[76:79], a[152:155], v[248:251], v[76:79]\n"
    "ds_read_b128 a[132:135], v2 offset:33280\n"
    "v_mfma_f32_16x16x32_bf16 v[76:79], a[156:159], v[252:255], v[76:79]\n"
    "v_exp_f32 v57, v57\n"
    "s_waitcnt lgkmcnt(2)\n"
    "v_mfma_f32_16x16x32_bf16 a[0:3], a[160:163], v[80:83], a[0:3]\n"
    "buffer_load_dwordx4 v6, s[40:43], s69 offen offset:1024 lds\n"
    "v_cvt_pk_bf16_f32 v100, v56, v57\n"
    "v_mfma_f32_16x16x32_bf16 a[4:7], a[164:167], v[80:83], a[4:7]\n"
    "v_exp_f32 v58, v58\n"
    "v_mfma_f32_16x16x32_bf16 a[8:11], a[168:171], v[80:83], a[8:11]\n"
    "ds_read_b128 a[136:139], v2 offset:33792\n"
    "v_mfma_f32_16x16x32_bf16 a[12:15], a[172:175], v[80:83], a[12:15]\n"
    "v_exp_f32 v59, v59\n"
    "v_mfma_f32_16x16x32_bf16 a[16:19], a[176:179], v[80:83], a[16:19]\n"
    "buffer_load_dwordx4 v7, s[40:43], s69 offen offset:2048 lds\n"
    "v_cvt_pk_bf16_f32 v101, v58, v59\n"
    "v_mfma_f32_16x16x32_bf16 a[20:23], a[180:183], v[80:83], a[20:23]\n"
    "v_exp_f32 v60, v60\n"
    "v_mfma_f32_16x16x32_bf16 a[24:27], a[184:187], v[80:83], a[24:27]\n"
    "ds_read_b128 a[140:143], v2 offset:34304\n"
    "v_mfma_f32_16x16x32_bf16 a[28:31], a[188:191], v[80:83], a[28:31]\n"
    "v_exp_f32 v61, v61\n"
    "v_mfma_f32_16x16x32_bf16 a[208:211], a[232:235], v[80:83], a[208:211]\n"
    "v_mfma_f32_16x16x32_bf16 a[32:35], a[160:163], v[84:87], a[32:35]\n"
    "buffer_load_dwordx4 v8, s[40:43], s69 offen offset:3072 lds\n"
    "v_cvt_pk_bf16_f32 v102, v60, v61\n"
    "v_mfma_f32_16x16x32_bf16 a[36:39], a[164:167], v[84:87], a[36:39]\n"
    "v_exp_f32 v62, v62\n"
    "v_mfma_f32_16x16x32_bf16 a[40:43], a[168:171], v[84:87], a[40:43]\n"
    "ds_read_b128 a[144:147], v2 offset:36864\n"
    "s_add_u32 m0, s71, 0\n"
    "v_mfma_f32_16x16x32_bf16 a[44:47], a[172:175], v[84:87], a[44:47]\n"
    "v_exp_f32 v63, v63\n"
    "v_mfma_f32_16x16x32_bf16 a[48:51], a[176:179], v[84:87], a[48:51]\n"
    "buffer_load_dwordx4 v9, s[44:47], s69 offen offset:0 lds\n"
    "v_cvt_pk_bf16_f32 v103, v62, v63\n"
    "v_mfma_f32_16x16x32_bf16 a[52:55], a[180:183], v[84:87], a[52:55]\n"
    "v_exp_f32 v64, v64\n"
    "v_mfma_f32_16x16x32_bf16 a[56:59], a[184:187], v[84:87], a[56:59]\n"
    "ds_read_b128 a[148:151], v2 offset:37376\n"
    "v_mfma_f32_16x16x32_bf16 a[60:63], a[188:191], v[84:87], a[60:63]\n"
    "v_exp_f32 v65, v65\n"
    "v_mfma_f32_16x16x32_bf16 a[212:215], a[232:235], v[84:87], a[212:215]\n"
    "v_mfma_f32_16x16x32_bf16 a[64:67], a[160:163], v[88:91], a[64:67]\n"
    "buffer_load_dwordx4 v10, s[44:47], s69 offen offset:1024 lds\n"
    "v_cvt_pk_bf16_f32 v104, v64, v65\n"
    "v_mfma_f32_16x16x32_bf16 a[68:71], a[164:167], v[88:91], a[68:71]\n"
    "v_exp_f32 v66, v66\n"
    "v_mfma_f32_16x16x32_bf16 a[72:75], a[168:171], v[88:91], a[72:75]\n"
    "ds_read_b128 a[152:155], v2 offset:37888\n"
    "v_mfma_f32_16x16x32_bf16 a[76:79], a[172:175], v[88:91], a[76:79]\n"
    "v_exp_f32 v67, v67\n"
    "v_mfma_f32_16x16x32_bf16 a[80:83], a[176:179], v[88:91], a[80:83]\n"
    "buffer_load_dwordx4 v9, s[44:47], s69 offen offset:2048 lds\n"
    "v_cvt_pk_bf16_f32 v105, v66, v67\n"
    "v_mfma_f32_16x16x32_bf16 a[84:87], a[180:183], v[88:91], a[84:87]\n"
    "v_exp_f32 v68, v68\n"
    "v_mfma_f32_16x16x32_bf16 a[88:91], a[184:187], v[88:91], a[88:91]\n"
    "ds_read_b128 a[156:159], v2 offset:38400\n"
    "v_mfma_f32_16x16x32_bf16 a[92:95], a[188:191], v[88:91], a[92:95]\n"
    "v_exp_f32 v69, v69\n"
    "v_mfma_f32_16x16x32_bf16 a[216:219], a[232:235], v[88:91], a[216:219]\n"
    "v_mfma_f32_16x16x32_bf16 a[96:99], a[160:163], v[92:95], a[96:99]\n"
    "buffer_load_dwordx4 v10, s[44:47], s69 offen offset:3072 lds\n"
    "v_cvt_pk_bf16_f32 v106, v68, v69\n"
    "v_mfma_f32_16x16x32_bf16 a[100:103], a[164:167], v[92:95], a[100:103]\n"
    "v_exp_f32 v70, v70\n"
    "v_mfma_f32_16x16x32_bf16 a[104:107], a[168:171], v[92:95], a[104:107]\n"
    "ds_read_b64_tr_b16 a[160:161], v3 offset:24576\n"
    "v_mfma_f32_16x16x32_bf16 a[108:111], a[172:175], v[92:95], a[108:111]\n"
    "v_exp_f32 v71, v71\n"
    "v_mfma_f32_16x16x32_bf16 a[112:115], a[176:179], v[92:95], a[112:115]\n"
    "ds_read_b64_tr_b16 a[162:163], v3 offset:28672\n"
    "v_cvt_pk_bf16_f32 v107, v70, v71\n"
    "v_mfma_f32_16x16x32_bf16 a[116:119], a[180:183], v[92:95], a[116:119]\n"
    "v_exp_f32 v72, v72\n"
    "v_mfma_f32_16x16x32_bf16 a[120:123], a[184:187], v[92:95], a[120:123]\n"
    "ds_read_b64_tr_b16 a[164:165], v4 offset:24576\n"
    "v_mfma_f32_16x16x32_bf16 a[124:127], a[188:191], v[92:95], a[124:127]\n"
    "v_exp_f32 v73, v73\n"
    "v_mfma_f32_16x16x32_bf16 a[220:223], a[232:235], v[92:95], a[220:223]\n"
    "s_waitcnt lgkmcnt(3)\n"
    "v_mfma_f32_16x16x32_bf16 v[48:51], a[128:131], v[192:195], v[32:35]\n"
    "ds_read_b64_tr_b16 a[166:167], v4 offset:28672\n"
    "v_cvt_pk_bf16_f32 v108, v72, v73\n"
    "v_mfma_f32_16x16x32_bf16 v[48:51], a[132:135], v[196:199], v[48:51]\n"
    "v_exp_f32 v74, v74\n"
    "v_mfma_f32_16x16x32_bf16 v[48:51], a[136:139], v[200:203], v[48:51]\n"
    "ds_read_b64_tr_b16 a[168:169], v3 offset:25088\n"
    "v_mfma_f32_16x16x32_bf16 v[48:51], a[140:143], v[204:207], v[48:51]\n"
    "v_exp_f32 v75, v75\n"
    "v_mfma_f32_16x16x32_bf16 v[52:55], a[144:147], v[192:195], v[32:35]\n"
    "ds_read_b64_tr_b16 a[170:171], v3 offset:29184\n"
    "v_cvt_pk_bf16_f32 v109, v74, v75\n"
    "v_mfma_f32_16x16x32_bf16 v[52:55], a[148:151], v[196:199], v[52:55]\n"
    "v_exp_f32 v76, v76\n"
    "v_mfma_f32_16x16x32_bf16 v[52:55], a[152:155], v[200:203], v[52:55]\n"
    "ds_read_b64_tr_b16 a[172:173], v4 offset:25088\n"
    "v_mfma_f32_16x16x32_bf16 v[52:55], a[156:159], v[204:207], v[52:55]\n"
    "v_exp_f32 v77, v77\n"
    "v_mfma_f32_16x16x32_bf16 v[56:59], a[128:131], v[208:211], v[36:39]\n"
    "ds_read_b64_tr_b16 a[174:175], v4 offset:29184\n"
    "v_cvt_pk_bf16_f32 v110, v76, v77\n"
    "v_mfma_f32_16x16x32_bf16 v[56:59], a[132:135], v[212:215], v[56:59]\n"
    "v_exp_f32 v78, v78\n"
    "v_mfma_f32_16x16x32_bf16 v[56:59], a[136:139], v[216:219], v[56:59]\n"
    "ds_read_b64_tr_b16 a[176:177], v3 offset:25600\n"
    "v_mfma_f32_16x16x32_bf16 v[56:59], a[140:143], v[220:223], v[56:59]\n"
    "v_exp_f32 v79, v79\n"
    "v_mfma_f32_16x16x32_bf16 v[60:63], a[144:147], v[208:211], v[36:39]\n"
    "ds_read_b64_tr_b16 a[178:179], v3 offset:29696\n"
    "v_cvt_pk_bf16_f32 v111, v78, v79\n"
    "v_mfma_f32_16x16x32_bf16 v[60:63], a[148:151], v[212:215], v[60:63]\n"
    "v_exp_f32 v48, v48\n"
    "v_mfma_f32_16x16x32_bf16 v[60:63], a[152:155], v[216:219], v[60:63]\n"
    "ds_read_b64_tr_b16 a[180:181], v4 offset:25600\n"
    "v_mfma_f32_16x16x32_bf16 v[60:63], a[156:159], v[220:223], v[60:63]\n"
    "v_exp_f32 v49, v49\n"
    "v_mfma_f32_16x16x32_bf16 v[64:67], a[128:131], v[224:227], v[40:43]\n"
    "ds_read_b64_tr_b16 a[182:183], v4 offset:29696\n"
    "v_cvt_pk_bf16_f32 v80, v48, v49\n"
    "v_mfma_f32_16x16x32_bf16 v[64:67], a[132:135], v[228:231], v[64:67]\n"
    "v_exp_f32 v50, v50\n"
    "v_mfma_f32_16x16x32_bf16 v[64:67], a[136:139], v[232:235], v[64:67]\n"
    "ds_read_b64_tr_b16 a[184:185], v3 offset:26112\n"
    "v_mfma_f32_16x16x32_bf16 v[64:67], a[140:143], v[236:239], v[64:67]\n"
    "v_exp_f32 v51, v51\n"
    "v_mfma_f32_16x16x32_bf16 v[68:71], a[144:147], v[224:227], v[40:43]\n"
    "ds_read_b64_tr_b16 a[186:187], v3 offset:30208\n"
    "v_cvt_pk_bf16_f32 v81, v50, v51\n"
    "v_mfma_f32_16x16x32_bf16 v[68:71], a[148:151], v[228:231], v[68:71]\n"
    "v_exp_f32 v52, v52\n"
    "v_mfma_f32_16x16x32_bf16 v[68:71], a[152:155], v[232:235], v[68:71]\n"
    "ds_read_b64_tr_b16 a[188:189], v4 offset:26112\n"
    "v_mfma_f32_16x16x32_bf16 v[68:71], a[156:159], v[236:239], v[68:71]\n"
    "v_exp_f32 v53, v53\n"
    "v_mfma_f32_16x16x32_bf16 v[72:75], a[128:131], v[240:243], v[44:47]\n"
    "ds_read_b64_tr_b16 a[190:191], v4 offset:30208\n"
    "v_cvt_pk_bf16_f32 v82, v52, v53\n"
    "v_mfma_f32_16x16x32_bf16 v[72:75], a[132:135], v[244:247], v[72:75]\n"
    "v_exp_f32 v54, v54\n"
    "v_mfma_f32_16x16x32_bf16 v[72:75], a[136:139], v[248:251], v[72:75]\n"
    "ds_read_b128 a[128:131], v2 offset:40960\n"
    "v_mfma_f32_16x16x32_bf16 v[72:75], a[140:143], v[252:255], v[72:75]\n"
    "v_exp_f32 v55, v55\n"
    "v_mfma_f32_16x16x32_bf16 v[76:79], a[144:147], v[240:243], v[44:47]\n"
    "v_cvt_pk_bf16_f32 v83, v54, v55\n"
    "v_mfma_f32_16x16x32_bf16 v[76:79], a[148:151], v[244:247], v[76:79]\n"
    "v_exp_f32 v56, v56\n"
    "v_mfma_f32_16x16x32_bf16 v[76:79], a[152:155], v[248:251], v[76:79]\n"
    "ds_read_b128 a[132:135], v2 offset:41472\n"
    "v_mfma_f32_16x16x32_bf16 v[76:79], a[156:159], v[252:255], v[76:79]\n"
    "v_exp_f32 v57, v57\n"
    "s_waitcnt lgkmcnt(2)\n"
    "v_mfma_f32_16x16x32_bf16 a[0:3], a[160:163], v[96:99], a[0:3]\n"
    "v_cvt_pk_bf16_f32 v84, v56, v57\n"
    "v_mfma_f32_16x16x32_bf16 a[4:7], a[164:167], v[96:99], a[4:7]\n"
    "v_exp_f32 v58, v58\n"
    "v_mfma_f32_16x16x32_bf16 a[8:11], a[168:171], v[96:99], a[8:11]\n"
    "ds_read_b128 a[136:139], v2 offset:41984\n"
    "v_mfma_f32_16x16x32_bf16 a[12:15], a[172:175], v[96:99], a[12:15]\n"
    "v_exp_f32 v59, v59\n"
    "v_mfma_f32_16x16x32_bf16 a[16:19], a[176:179], v[96:99], a[16:19]\n"
    "v_cvt_pk_bf16_f32 v85, v58, v59\n"
    "v_mfma_f32_16x16x32_bf16 a[20:23], a[180:183], v[96:99], a[20:23]\n"
    "v_exp_f32 v60, v60\n"
    "v_mfma_f32_16x16x32_bf16 a[24:27], a[184:187], v[96:99], a[24:27]\n"
    "ds_read_b128 a[140:143], v2 offset:42496\n"
    "v_mfma_f32_16x16x32_bf16 a[28:31], a[188:191], v[96:99], a[28:31]\n"
    "v_exp_f32 v61, v61\n"
    "v_mfma_f32_16x16x32_bf16 a[208:211], a[232:235], v[96:99], a[208:211]\n"
    "v_mfma_f32_16x16x32_bf16 a[32:35], a[160:163], v[100:103], a[32:35]\n"
    "v_cvt_pk_bf16_f32 v86, v60, v61\n"
    "v_mfma_f32_16x16x32_bf16 a[36:39], a[164:167], v[100:103], a[36:39]\n"
    "v_exp_f32 v62, v62\n"
    "v_mfma_f32_16x16x32_bf16 a[40:43], a[168:171], v[100:103], a[40:43]\n"
    "ds_read_b128 a[144:147], v2 offset:45056\n"
    "v_mfma_f32_16x16x32_bf16 a[44:47], a[172:175], v[100:103], a[44:47]\n"
    "v_exp_f32 v63, v63\n"
    "v_mfma_f32_16x16x32_bf16 a[48:51], a[176:179], v[100:103], a[48:51]\n"
    "v_cvt_pk_bf16_f32 v87, v62, v63\n"
    "v_mfma_f32_16x16x32_bf16 a[52:55], a[180:183], v[100:103], a[52:55]\n"
    "v_exp_f32 v64, v64\n"
    "v_mfma_f32_16x16x32_bf16 a[56:59], a[184:187], v[100:103], a[56:59]\n"
    "ds_read_b128 a[148:151], v2 offset:45568\n"
    "v_mfma_f32_16x16x32_bf16 a[60:63], a[188:191], v[100:103], a[60:63]\n"
    "v_exp_f32 v65, v65\n"
    "v_mfma_f32_16x16x32_bf16 a[212:215], a[232:235], v[100:103], a[212:215]\n"
    "v_mfma_f32_16x16x32_bf16 a[64:67], a[160:163], v[104:107], a[64:67]\n"
    "v_cvt_pk_bf16_f32 v88, v64, v65\n"
    "v_mfma_f32_16x16x32_bf16 a[68:71], a[164:167], v[104:107], a[68:71]\n"
    "v_exp_f32 v66, v66\n"
    "v_mfma_f32_16x16x32_bf16 a[72:75], a[168:171], v[104:107], a[72:75]\n"
    "ds_read_b128 a[152:155], v2 offset:46080\n"
    "v_mfma_f32_16x16x32_bf16 a[76:79], a[172:175], v[104:107], a[76:79]\n"
    "v_exp_f32 v67, v67\n"
    "v_mfma_f32_16x16x32_bf16 a[80:83], a[176:179], v[104:107], a[80:83]\n"
    "v_cvt_pk_bf16_f32 v89, v66, v67\n"
    "v_mfma_f32_16x16x32_bf16 a[84:87], a[180:183], v[104:107], a[84:87]\n"
    "v_exp_f32 v68, v68\n"
    "v_mfma_f32_16x16x32_bf16 a[88:91], a[184:187], v[104:107], a[88:91]\n"
    "ds_read_b128 a[156:159], v2 offset:46592\n"
    "v_mfma_f32_16x16x32_bf16 a[92:95], a[188:191], v[104:107], a[92:95]\n"
    "v_exp_f32 v69, v69\n"
    "v_mfma_f32_16x16x32_bf16 a[216:219], a[232:235], v[104:107], a[216:219]\n"
    "v_mfma_f32_16x16x32_bf16 a[96:99], a[160:163], v[108:111], a[96:99]\n"
    "v_cvt_pk_bf16_f32 v90, v68, v69\n"
    "s_waitcnt vmcnt(12)\n"
    "s_barrier\n"
    "v_mfma_f32_16x16x32_bf16 a[100:103], a[164:167], v[108:111], a[100:103]\n"
    "v_exp_f32 v70, v70\n"
    "v_mfma_f32_16x16x32_bf16 a[104:107], a[168:171], v[108:111], a[104:107]\n"
    "ds_read_b64_tr_b16 a[160:161], v3 offset:32768\n"
    "v_mfma_f32_16x16x32_bf16 a[108:111], a[172:175], v[108:111], a[108:111]\n"
    "v_exp_f32 v71, v71\n"
    "v_mfma_f32_16x16x32_bf16 a[112:115], a[176:179], v[108:111], a[112:115]\n"
    "ds_read_b64_tr_b16 a[162:163], v3 offset:36864\n"
    "v_cvt_pk_bf16_f32 v91, v70, v71\n"
    "v_mfma_f32_16x16x32_bf16 a[116:119], a[180:183], v[108:111], a[116:119]\n"
    "v_exp_f32 v72, v72\n"
    "v_mfma_f32_16x16x32_bf16 a[120:123], a[184:187], v[108:111], a[120:123]\n"
    "ds_read_b64_tr_b16 a[164:165], v4 offset:32768\n"
    "v_mfma_f32_16x16x32_bf16 a[124:127], a[188:191], v[108:111], a[124:127]\n"
    "v_exp_f32 v73, v73\n"
    "v_mfma_f32_16x16x32_bf16 a[220:223], a[232:235], v[108:111], a[220:223]\n"
    "s_waitcnt lgkmcnt(3)\n"
    "v_mfma_f32_16x16x32_bf16 v[48:51], a[128:131], v[192:195], v[32:35]\n"
    "ds_read_b64_tr_b16 a[166:167], v4 offset:36864\n"
    "v_cvt_pk_bf16_f32 v92, v72, v73\n"
    "v_mfma_f32_16x16x32_bf16 v[48:51], a[132:135], v[196:199], v[48:51]\n"
    "v_exp_f32 v74, v74\n"
    "v_mfma_f32_16x16x32_bf16 v[48:51], a[136:139], v[200:203], v[48:51]\n"
    "ds_read_b64_tr_b16 a[168:169], v3 offset:33280\n"
    "s_add_u32 s68, s68, 16384\n"
    "v_mfma_f32_16x16x32_bf16 v[48:51], a[140:143], v[204:207], v[48:51]\n"
    "v_exp_f32 v75, v75\n"
    "v_mfma_f32_16x16x32_bf16 v[52:55], a[144:147], v[192:195], v[32:35]\n"
    "ds_read_b64_tr_b16 a[170:171], v3 offset:37376\n"
    "v_cvt_pk_bf16_f32 v93, v74, v75\n"
    "v_mfma_f32_16x16x32_bf16 v[52:55], a[148:151], v[196:199], v[52:55]\n"
    "v_exp_f32 v76, v76\n"
    "v_mfma_f32_16x16x32_bf16 v[52:55], a[152:155], v[200:203], v[52:55]\n"
    "ds_read_b64_tr_b16 a[172:173], v4 offset:33280\n"
    "s_and_b32 s69, s68, 0x7ffff\n"
    "v_mfma_f32_16x16x32_bf16 v[52:55], a[156:159], v[204:207], v[52:55]\n"
    "v_exp_f32 v77, v77\n"
    "v_mfma_f32_16x16x32_bf16 v[56:59], a[128:131], v[208:211], v[36:39]\n"
    "ds_read_b64_tr_b16 a[174:175], v4 offset:37376\n"
    "v_cvt_pk_bf16_f32 v94, v76, v77\n"
    "v_mfma_f32_16x16x32_bf16 v[56:59], a[132:135], v[212:215], v[56:59]\n"
    "v_exp_f32 v78, v78\n"
    "v_mfma_f32_16x16x32_bf16 v[56:59], a[136:139], v[216:219], v[56:59]\n"
    "ds_read_b64_tr_b16 a[176:177], v3 offset:33792\n"
    "v_mfma_f32_16x16x32_bf16 v[56:59], a[140:143], v[220:223], v[56:59]\n"
    "v_exp_f32 v79, v79\n"
    "v_mfma_f32_16x16x32_bf16 v[60:63], a[144:147], v[208:211], v[36:39]\n"
    "ds_read_b64_tr_b16 a[178:179], v3 offset:37888\n"
    "v_cvt_pk_bf16_f32 v95, v78, v79\n"
    "v_mfma_f32_16x16x32_bf16 v[60:63], a[148:151], v[212:215], v[60:63]\n"
    "v_exp_f32 v48, v48\n"
    "v_mfma_f32_16x16x32_bf16 v[60:63], a[152:155], v[216:219], v[60:63]\n"
    "ds_read_b64_tr_b16 a[180:181], v4 offset:33792\n"
    "v_mfma_f32_16x16x32_bf16 v[60:63], a[156:159], v[220:223], v[60:63]\n"
    "v_exp_f32 v49, v49\n"
    "v_mfma_f32_16x16x32_bf16 v[64:67], a[128:131], v[224:227], v[40:43]\n"
    "ds_read_b64_tr_b16 a[182:183], v4 offset:37888\n"
    "v_cvt_pk_bf16_f32 v96, v48, v49\n"
    "v_mfma_f32_16x16x32_bf16 v[64:67], a[132:135], v[228:231], v[64:67]\n"
    "v_exp_f32 v50, v50\n"
    "v_mfma_f32_16x16x32_bf16 v[64:67], a[136:139], v[232:235], v[64:67]\n"
    "ds_read_b64_tr_b16 a[184:185], v3 offset:34304\n"
    "v_mfma_f32_16x16x32_bf16 v[64:67], a[140:143], v[236:239], v[64:67]\n"
    "v_exp_f32 v51, v51\n"
    "v_mfma_f32_16x16x32_bf16 v[68:71], a[144:147], v[224:227], v[40:43]\n"
    "ds_read_b64_tr_b16 a[186:187], v3 offset:38400\n"
    "v_cvt_pk_bf16_f32 v97, v50, v51\n"
    "v_mfma_f32_16x16x32_bf16 v[68:71], a[148:151], v[228:231], v[68:71]\n"
    "v_exp_f32 v52, v52\n"
    "v_mfma_f32_16x16x32_bf16 v[68:71], a[152:155], v[232:235], v[68:71]\n"
    "ds_read_b64_tr_b16 a[188:189], v4 offset:34304\n"
    "v_mfma_f32_16x16x32_bf16 v[68:71], a[156:159], v[236:239], v[68:71]\n"
    "v_exp_f32 v53, v53\n"
    "v_mfma_f32_16x16x32_bf16 v[72:75], a[128:131], v[240:243], v[44:47]\n"
    "ds_read_b64_tr_b16 a[190:191], v4 offset:38400\n"
    "v_cvt_pk_bf16_f32 v98, v52, v53\n"
    "v_mfma_f32_16x16x32_bf16 v[72:75], a[132:135], v[244:247], v[72:75]\n"
    "v_exp_f32 v54, v54\n"
    "v_mfma_f32_16x16x32_bf16 v[72:75], a[136:139], v[248:251], v[72:75]\n"
    "ds_read_b128 a[128:131], v2 offset:49152\n"
    "s_add_u32 m0, s70, 16384\n"
    "v_mfma_f32_16x16x32_bf16 v[72:75], a[140:143], v[252:255], v[72:75]\n"
    "v_exp_f32 v55, v55\n"
    "v_mfma_f32_16x16x32_bf16 v[76:79], a[144:147], v[240:243], v[44:47]\n"
    "buffer_load_dwordx4 v5, s[40:43], s69 offen offset:0 lds\n"
    "v_cvt_pk_bf16_f32 v99, v54, v55\n"
    "v_mfma_f32_16x16x32_bf16 v[76:79], a[148:151], v[244:247], v[76:79]\n"
    "v_exp_f32 v56, v56\n"
    "v_mfma_f32_16x16x32_bf16 v[76:79], a[152:155], v[248:251], v[76:79]\n"
    "ds_read_b128 a[132:135], v2 offset:49664\n"
    "v_mfma_f32_16x16x32_bf16 v[76:79], a[156:159], v[252:255], v[76:79]\n"
    "v_exp_f32 v57, v57\n"
    "s_waitcnt lgkmcnt(2)\n"
    "v_mfma_f32_16x16x32_bf16 a[0:3], a[160:163], v[80:83], a[0:3]\n"
    "buffer_load_dwordx4 v6, s[40:43], s69 offen offset:1024 lds\n"
    "v_cvt_pk_bf16_f32 v100, v56, v57\n"
    "v_mfma_f32_16x16x32_bf16 a[4:7], a[164:167], v[80:83], a[4:7]\n"
    "v_exp_f32 v58, v58\n"
    "v_mfma_f32_16x16x32_bf16 a[8:11], a[168:171], v[80:83], a[8:11]\n"
    "ds_read_b128 a[136:139], v2 offset:50176\n"
    "v_mfma_f32_16x16x32_bf16 a[12:15], a[172:175], v[80:83], a[12:15]\n"
    "v_exp_f32 v59, v59\n"
    "v_mfma_f32_16x16x32_bf16 a[16:19], a[176:179], v[80:83], a[16:19]\n"
    "buffer_load_dwordx4 v7, s[40:43], s69 offen offset:2048 lds\n"
    "v_cvt_pk_bf16_f32 v101, v58, v59\n"
    "v_mfma_f32_16x16x32_bf16 a[20:23], a[180:183], v[80:83], a[20:23]\n"
    "v_exp_f32 v60, v60\n"
    "v_mfma_f32_16x16x32_bf16 a[24:27], a[184:187], v[80:83], a[24:27]\n"
    "ds_read_b128 a[140:143], v2 offset:50688\n"
    "v_mfma_f32_16x16x32_bf16 a[28:31], a[188:191], v[80:83], a[28:31]\n"
    "v_exp_f32 v61, v61\n"
    "v_mfma_f32_16x16x32_bf16 a[208:211], a[232:235], v[80:83], a[208:211]\n"
    "v_mfma_f32_16x16x32_bf16 a[32:35], a[160:163], v[84:87], a[32:35]\n"
    "buffer_load_dwordx4 v8, s[40:43], s69 offen offset:3072 lds\n"
    "v_cvt_pk_bf16_f32 v102, v60, v61\n"
    "v_mfma_f32_16x16x32_bf16 a[36:39], a[164:167], v[84:87], a[36:39]\n"
    "v_exp_f32 v62, v62\n"
    "v_mfma_f32_16x16x32_bf16 a[40:43], a[168:171], v[84:87], a[40:43]\n"
    "ds_read_b128 a[144:147], v2 offset:53248\n"
    "s_add_u32 m0, s71, 16384\n"
    "v_mfma_f32_16x16x32_bf16 a[44:47], a[172:175], v[84:87], a[44:47]\n"
    "v_exp_f32 v63, v63\n"
    "v_mfma_f32_16x16x32_bf16 a[48:51], a[176:179], v[84:87], a[48:51]\n"
    "buffer_load_dwordx4 v9, s[44:47], s69 offen offset:0 lds\n"
    "v_cvt_pk_bf16_f32 v103, v62, v63\n"
    "v_mfma_f32_16x16x32_bf16 a[52:55], a[180:183], v[84:87], a[52:55]\n"
    "v_exp_f32 v64, v64\n"
    "v_mfma_f32_16x16x32_bf16 a[56:59], a[184:187], v[84:87], a[56:59]\n"
    "ds_read_b128 a[148:151], v2 offset:53760\n"
    "v_mfma_f32_16x16x32_bf16 a[60:63], a[188:191], v[84:87], a[60:63]\n"
    "v_exp_f32 v65, v65\n"
    "v_mfma_f32_16x16x32_bf16 a[212:215], a[232:235], v[84:87], a[212:215]\n"
    "v_mfma_f32_16x16x32_bf16 a[64:67], a[160:163], v[88:91], a[64:67]\n"
    "buffer_load_dwordx4 v10, s[44:47], s69 offen offset:1024 lds\n"
    "v_cvt_pk_bf16_f32 v104, v64, v65\n"
    "v_mfma_f32_16x16x32_bf16 a[68:71], a[164:167], v[88:91], a[68:71]\n"
    "v_exp_f32 v66, v66\n"
    "v_mfma_f32_16x16x32_bf16 a[72:75], a[168:171], v[88:91], a[72:75]\n"
    "ds_read_b128 a[152:155], v2 offset:54272\n"
    "v_mfma_f32_16x16x32_bf16 a[76:79], a[172:175], v[88:91], a[76:79]\n"
    "v_exp_f32 v67, v67\n"
    "v_mfma_f32_16x16x32_bf16 a[80:83], a[176:179], v[88:91], a[80:83]\n"
    "buffer_load_dwordx4 v9, s[44:47], s69 offen offset:2048 lds\n"
    "v_cvt_pk_bf16_f32 v105, v66, v67\n"
    "v_mfma_f32_16x16x32_bf16 a[84:87], a[180:183], v[88:91], a[84:87]\n"
    "v_exp_f32 v68, v68\n"
    "v_mfma_f32_16x16x32_bf16 a[88:91], a[184:187], v[88:91], a[88:91]\n"
    "ds_read_b128 a[156:159], v2 offset:54784\n"
    "v_mfma_f32_16x16x32_bf16 a[92:95], a[188:191], v[88:91], a[92:95]\n"
    "v_exp_f32 v69, v69\n"
    "v_mfma_f32_16x16x32_bf16 a[216:219], a[232:235], v[88:91], a[216:219]\n"
    "v_mfma_f32_16x16x32_bf16 a[96:99], a[160:163], v[92:95], a[96:99]\n"
    "buffer_load_dwordx4 v10, s[44:47], s69 offen offset:3072 lds\n"
    "v_cvt_pk_bf16_f32 v106, v68, v69\n"
    "v_mfma_f32_16x16x32_bf16 a[100:103], a[164:167], v[92:95], a[100:103]\n"
    "v_exp_f32 v70, v70\n"
    "v_mfma_f32_16x16x32_bf16 a[104:107], a[168:171], v[92:95], a[104:107]\n"
    "ds_read_b64_tr_b16 a[160:161], v3 offset:40960\n"
    "v_mfma_f32_16x16x32_bf16 a[108:111], a[172:175], v[92:95], a[108:111]\n"
    "v_exp_f32 v71, v71\n"
    "v_mfma_f32_16x16x32_bf16 a[112:115], a[176:179], v[92:95], a[112:115]\n"
    "ds_read_b64_tr_b16 a[162:163], v3 offset:45056\n"
    "v_cvt_pk_bf16_f32 v107, v70, v71\n"
    "v_mfma_f32_16x16x32_bf16 a[116:119], a[180:183], v[92:95], a[116:119]\n"
    "v_exp_f32 v72, v72\n"
    "v_mfma_f32_16x16x32_bf16 a[120:123], a[184:187], v[92:95], a[120:123]\n"
    "ds_read_b64_tr_b16 a[164:165], v4 offset:40960\n"
    "v_mfma_f32_16x16x32_bf16 a[124:127], a[188:191], v[92:95], a[124:127]\n"
    "v_exp_f32 v73, v73\n"
    "v_mfma_f32_16x16x32_bf16 a[220:223], a[232:235], v[92:95], a[220:223]\n"
    "s_waitcnt lgkmcnt(3)\n"
    "v_mfma_f32_16x16x32_bf16 v[48:51], a[128:131], v[192:195], v[32:35]\n"
    "ds_read_b64_tr_b16 a[166:167], v4 offset:45056\n"
    "v_cvt_pk_bf16_f32 v108, v72, v73\n"
    "v_mfma_f32_16x16x32_bf16 v[48:51], a[132:135], v[196:199], v[48:51]\n"
    "v_exp_f32 v74, v74\n"
    "v_mfma_f32_16x16x32_bf16 v[48:51], a[136:139], v[200:203], v[48:51]\n"
    "ds_read_b64_tr_b16 a[168:169], v3 offset:41472\n"
    "v_mfma_f32_16x16x32_bf16 v[48:51], a[140:143], v[204:207], v[48:51]\n"
    "v_exp_f32 v75, v75\n"
    "v_mfma_f32_16x16x32_bf16 v[52:55], a[144:147], v[192:195], v[32:35]\n"
    "ds_read_b64_tr_b16 a[170:171], v3 offset:45568\n"
    "v_cvt_pk_bf16_f32 v109, v74, v75\n"
    "v_mfma_f32_16x16x32_bf16 v[52:55], a[148:151], v[196:199], v[52:55]\n"
    "v_exp_f32 v76, v76\n"
    "v_mfma_f32_16x16x32_bf16 v[52:55], a[152:155], v[200:203], v[52:55]\n"
    "ds_read_b64_tr_b16 a[172:173], v4 offset:41472\n"
    "v_mfma_f32_16x16x32_bf16 v[52:55], a[156:159], v[204:207], v[52:55]\n"
    "v_exp_f32 v77, v77\n"
    "v_mfma_f32_16x16x32_bf16 v[56:59], a[128:131], v[208:211], v[36:39]\n"
    "ds_read_b64_tr_b16 a[174:175], v4 offset:45568\n"
    "v_cvt_pk_bf16_f32 v110, v76, v77\n"
    "v_mfma_f32_16x16x32_bf16 v[56:59], a[132:135], v[212:215], v[56:59]\n"
    "v_exp_f32 v78, v78\n"
    "v_mfma_f32_16x16x32_bf16 v[56:59], a[136:139], v[216:219], v[56:59]\n"
    "ds_read_b64_tr_b16 a[176:177], v3 offset:41984\n"
    "v_mfma_f32_16x16x32_bf16 v[56:59], a[140:143], v[220:223], v[56:59]\n"
    "v_exp_f32 v79, v79\n"
    "v_mfma_f32_16x16x32_bf16 v[60:63], a[144:147], v[208:211], v[36:39]\n"
    "ds_read_b64_tr_b16 a[178:179], v3 offset:46080\n"
    "v_cvt_pk_bf16_f32 v111, v78, v79\n"
    "v_mfma_f32_16x16x32_bf16 v[60:63], a[148:151], v[212:215], v[60:63]\n"
    "v_exp_f32 v48, v48\n"
    "v_mfma_f32_16x16x32_bf16 v[60:63], a[152:155], v[216:219], v[60:63]\n"
    "ds_read_b64_tr_b16 a[180:181], v4 offset:41984\n"
    "v_mfma_f32_16x16x32_bf16 v[60:63], a[156:159], v[220:223], v[60:63]\n"
    "v_exp_f32 v49, v49\n"
    "v_mfma_f32_16x16x32_bf16 v[64:67], a[128:131], v[224:227], v[40:43]\n"
    "ds_read_b64_tr_b16 a[182:183], v4 offset:46080\n"
    "v_cvt_pk_bf16_f32 v80, v48, v49\n"
    "v_mfma_f32_16x16x32_bf16 v[64:67], a[132:135], v[228:231], v[64:67]\n"
    "v_exp_f32 v50, v50\n"
    "v_mfma_f32_16x16x32_bf16 v[64:67], a[136:139], v[232:235], v[64:67]\n"
    "ds_read_b64_tr_b16 a[184:185], v3 offset:42496\n"
    "v_mfma_f32_16x16x32_bf16 v[64:67], a[140:143], v[236:239], v[64:67]\n"
    "v_exp_f32 v51, v51\n"
    "v_mfma_f32_16x16x32_bf16 v[68:71], a[144:147], v[224:227], v[40:43]\n"
    "ds_read_b64_tr_b16 a[186:187], v3 offset:46592\n"
    "v_cvt_pk_bf16_f32 v81, v50, v51\n"
    "v_mfma_f32_16x16x32_bf16 v[68:71], a[148:151], v[228:231], v[68:71]\n"
    "v_exp_f32 v52, v52\n"
    "v_mfma_f32_16x16x32_bf16 v[68:71], a[152:155], v[232:235], v[68:71]\n"
    "ds_read_b64_tr_b16 a[188:189], v4 offset:42496\n"
    "v_mfma_f32_16x16x32_bf16 v[68:71], a[156:159], v[236:239], v[68:71]\n"
    "v_exp_f32 v53, v53\n"
    "v_mfma_f32_16x16x32_bf16 v[72:75], a[128:131], v[240:243], v[44:47]\n"
    "ds_read_b64_tr_b16 a[190:191], v4 offset:46592\n"
    "v_cvt_pk_bf16_f32 v82, v52, v53\n"
    "v_mfma_f32_16x16x32_bf16 v[72:75], a[132:135], v[244:247], v[72:75]\n"
    "v_exp_f32 v54, v54\n"
    "v_mfma_f32_16x16x32_bf16 v[72:75], a[136:139], v[248:251], v[72:75]\n"
    "ds_read_b128 a[128:131], v2 offset:57344\n"
    "v_mfma_f32_16x16x32_bf16 v[72:75], a[140:143], v[252:255], v[72:75]\n"
    "v_exp_f32 v55, v55\n"
    "v_mfma_f32_16x16x32_bf16 v[76:79], a[144:147], v[240:243], v[44:47]\n"
    "v_cvt_pk_bf16_f32 v83, v54, v55\n"
    "v_mfma_f32_16x16x32_bf16 v[76:79], a[148:151], v[244:247], v[76:79]\n"
    "v_exp_f32 v56, v56\n"
    "v_mfma_f32_16x16x32_bf16 v[76:79], a[152:155], v[248:251], v[76:79]\n"
    "ds_read_b128 a[132:135], v2 offset:57856\n"
    "v_mfma_f32_16x16x32_bf16 v[76:79], a[156:159], v[252:255], v[76:79]\n"
    "v_exp_f32 v57, v57\n"
    "s_waitcnt lgkmcnt(2)\n"
    "v_mfma_f32_16x16x32_bf16 a[0:3], a[160:163], v[96:99], a[0:3]\n"
    "v_cvt_pk_bf16_f32 v84, v56, v57\n"
    "v_mfma_f32_16x16x32_bf16 a[4:7], a[164:167], v[96:99], a[4:7]\n"
    "v_exp_f32 v58, v58\n"
    "v_mfma_f32_16x16x32_bf16 a[8:11], a[168:171], v[96:99], a[8:11]\n"
    "ds_read_b128 a[136:139], v2 offset:58368\n"
    "v_mfma_f32_16x16x32_bf16 a[12:15], a[172:175], v[96:99], a[12:15]\n"
    "v_exp_f32 v59, v59\n"
    "v_mfma_f32_16x16x32_bf16 a[16:19], a[176:179], v[96:99], a[16:19]\n"
    "v_cvt_pk_bf16_f32 v85, v58, v59\n"
    "v_mfma_f32_16x16x32_bf16 a[20:23], a[180:183], v[96:99], a[20:23]\n"
    "v_exp_f32 v60, v60\n"
    "v_mfma_f32_16x16x32_bf16 a[24:27], a[184:187], v[96:99], a[24:27]\n"
    "ds_read_b128 a[140:143], v2 offset:58880\n"
    "v_mfma_f32_16x16x32_bf16 a[28:31], a[188:191], v[96:99], a[28:31]\n"
    "v_exp_f32 v61, v61\n"
    "v_mfma_f32_16x16x32_bf16 a[208:211], a[232:235], v[96:99], a[208:211]\n"
    "v_mfma_f32_16x16x32_bf16 a[32:35], a[160:163], v[100:103], a[32:35]\n"
    "v_cvt_pk_bf16_f32 v86, v60, v61\n"
    "v_mfma_f32_16x16x32_bf16 a[36:39], a[164:167], v[100:103], a[36:39]\n"
    "v_exp_f32 v62, v62\n"
    "v_mfma_f32_16x16x32_bf16 a[40:43], a[168:171], v[100:103], a[40:43]\n"
    "ds_read_b128 a[144:147], v2 offset:61440\n"
    "v_mfma_f32_16x16x32_bf16 a[44:47], a[172:175], v[100:103], a[44:47]\n"
    "v_exp_f32 v63, v63\n"
    "v_mfma_f32_16x16x32_bf16 a[48:51], a[176:179], v[100:103], a[48:51]\n"
    "v_cvt_pk_bf16_f32 v87, v62, v63\n"
    "v_mfma_f32_16x16x32_bf16 a[52:55], a[180:183], v[100:103], a[52:55]\n"
    "v_exp_f32 v64, v64\n"
    "v_mfma_f32_16x16x32_bf16 a[56:59], a[184:187], v[100:103], a[56:59]\n"
    "ds_read_b128 a[148:151], v2 offset:61952\n"
    "v_mfma_f32_16x16x32_bf16 a[60:63], a[188:191], v[100:103], a[60:63]\n"
    "v_exp_f32 v65, v65\n"
    "v_mfma_f32_16x16x32_bf16 a[212:215], a[232:235], v[100:103], a[212:215]\n"
    "v_mfma_f32_16x16x32_bf16 a[64:67], a[160:163], v[104:107], a[64:67]\n"
    "v_cvt_pk_bf16_f32 v88, v64, v65\n"
    "v_mfma_f32_16x16x32_bf16 a[68:71], a[164:167], v[104:107], a[68:71]\n"
    "v_exp_f32 v66, v66\n"
    "v_mfma_f32_16x16x32_bf16 a[72:75], a[168:171], v[104:107], a[72:75]\n"
    "ds_read_b128 a[152:155], v2 offset:62464\n"
    "v_mfma_f32_16x16x32_bf16 a[76:79], a[172:175], v[104:107], a[76:79]\n"
    "v_exp_f32 v67, v67\n"
    "v_mfma_f32_16x16x32_bf16 a[80:83], a[176:179], v[104:107], a[80:83]\n"
    "v_cvt_pk_bf16_f32 v89, v66, v67\n"
    "v_mfma_f32_16x16x32_bf16 a[84:87], a[180:183], v[104:107], a[84:87]\n"
    "v_exp_f32 v68, v68\n"
    "v_mfma_f32_16x16x32_bf16 a[88:91], a[184:187], v[104:107], a[88:91]\n"
    "ds_read_b128 a[156:159], v2 offset:62976\n"
    "v_mfma_f32_16x16x32_bf16 a[92:95], a[188:191], v[104:107], a[92:95]\n"
    "v_exp_f32 v69, v69\n"
    "v_mfma_f32_16x16x32_bf16 a[216:219], a[232:235], v[104:107], a[216:219]\n"
    "v_mfma_f32_16x16x32_bf16 a[96:99], a[160:163], v[108:111], a[96:99]\n"
    "v_cvt_pk_bf16_f32 v90, v68, v69\n"
    "s_waitcnt vmcnt(12)\n"
    "s_barrier\n"
    "v_mfma_f32_16x16x32_bf16 a[100:103], a[164:167], v[108:111], a[100:103]\n"
    "v_exp_f32 v70, v70\n"
    "v_mfma_f32_16x16x32_bf16 a[104:107], a[168:171], v[108:111], a[104:107]\n"
    "ds_read_b64_tr_b16 a[160:161], v3 offset:49152\n"
    "v_mfma_f32_16x16x32_bf16 a[108:111], a[172:175], v[108:111], a[108:111]\n"
    "v_exp_f32 v71, v71\n"
    "v_mfma_f32_16x16x32_bf16 a[112:115], a[176:179], v[108:111], a[112:115]\n"
    "ds_read_b64_tr_b16 a[162:163], v3 offset:53248\n"
    "v_cvt_pk_bf16_f32 v91, v70, v71\n"
    "v_mfma_f32_16x16x32_bf16 a[116:119], a[180:183], v[108:111], a[116:119]\n"
    "v_exp_f32 v72, v72\n"
    "v_mfma_f32_16x16x32_bf16 a[120:123], a[184:187], v[108:111], a[120:123]\n"
    "ds_read_b64_tr_b16 a[164:165], v4 offset:49152\n"
    "v_mfma_f32_16x16x32_bf16 a[124:127], a[188:191], v[108:111], a[124:127]\n"
    "v_exp_f32 v73, v73\n"
    "v_mfma_f32_16x16x32_bf16 a[220:223], a[232:235], v[108:111], a[220:223]\n"
    "s_waitcnt lgkmcnt(3)\n"
    "v_mfma_f32_16x16x32_bf16 v[48:51], a[128:131], v[192:195], v[32:35]\n"
    "ds_read_b64_tr_b16 a[166:167], v4 offset:53248\n"
    "v_cvt_pk_bf16_f32 v92, v72, v73\n"
    "v_mfma_f32_16x16x32_bf16 v[48:51], a[132:135], v[196:199], v[48:51]\n"
    "v_exp_f32 v74, v74\n"
    "v_mfma_f32_16x16x32_bf16 v[48:51], a[136:139], v[200:203], v[48:51]\n"
    "ds_read_b64_tr_b16 a[168:169], v3 offset:49664\n"
    "s_add_u32 s68, s68, 16384\n"
    "v_mfma_f32_16x16x32_bf16 v[48:51], a[140:143], v[204:207], v[48:51]\n"
    "v_exp_f32 v75, v75\n"
    "v_mfma_f32_16x16x32_bf16 v[52:55], a[144:147], v[192:195], v[32:35]\n"
    "ds_read_b64_tr_b16 a[170:171], v3 offset:53760\n"
    "v_cvt_pk_bf16_f32 v93, v74, v75\n"
    "v_mfma_f32_16x16x32_bf16 v[52:55], a[148:151], v[196:199], v[52:55]\n"
    "v_exp_f32 v76, v76\n"
    "v_mfma_f32_16x16x32_bf16 v[52:55], a[152:155], v[200:203], v[52:55]\n"
    "ds_read_b64_tr_b16 a[172:173], v4 offset:49664\n"
    "s_and_b32 s69, s68, 0x7ffff\n"
    "v_mfma_f32_16x16x32_bf16 v[52:55], a[156:159], v[204:207], v[52:55]\n"
    "v_exp_f32 v77, v77\n"
    "v_mfma_f32_16x16x32_bf16 v[56:59], a[128:131], v[208:211], v[36:39]\n"
    "ds_read_b64_tr_b16 a[174:175], v4 offset:53760\n"
    "v_cvt_pk_bf16_f32 v94, v76, v77\n"
    "v_mfma_f32_16x16x32_bf16 v[56:59], a[132:135], v[212:215], v[56:59]\n"
    "v_exp_f32 v78, v78\n"
    "v_mfma_f32_16x16x32_bf16 v[56:59], a[136:139], v[216:219], v[56:59]\n"
    "ds_read_b64_tr_b16 a[176:177], v3 offset:50176\n"
    "v_mfma_f32_16x16x32_bf16 v[56:59], a[140:143], v[220:223], v[56:59]\n"
    "v_exp_f32 v79, v79\n"
    "v_mfma_f32_16x16x32_bf16 v[60:63], a[144:147], v[208:211], v[36:39]\n"
    "ds_read_b64_tr_b16 a[178:179], v3 offset:54272\n"
    "v_cvt_pk_bf16_f32 v95, v78, v79\n"
    "v_mfma_f32_16x16x32_bf16 v[60:63], a[148:151], v[212:215], v[60:63]\n"
    "v_exp_f32 v48, v48\n"
    "v_mfma_f32_16x16x32_bf16 v[60:63], a[152:155], v[216:219], v[60:63]\n"
    "ds_read_b64_tr_b16 a[180:181], v4 offset:50176\n"
    "v_mfma_f32_16x16x32_bf16 v[60:63], a[156:159], v[220:223], v[60:63]\n"
    "v_exp_f32 v49, v49\n"
    "v_mfma_f32_16x16x32_bf16 v[64:67], a[128:131], v[224:227], v[40:43]\n"
    "ds_read_b64_tr_b16 a[182:183], v4 offset:54272\n"
    "v_cvt_pk_bf16_f32 v96, v48, v49\n"
    "v_mfma_f32_16x16x32_bf16 v[64:67], a[132:135], v[228:231], v[64:67]\n"
    "v_exp_f32 v50, v50\n"
    "v_mfma_f32_16x16x32_bf16 v[64:67], a[136:139], v[232:235], v[64:67]\n"
    "ds_read_b64_tr_b16 a[184:185], v3 offset:50688\n"
    "v_mfma_f32_16x16x32_bf16 v[64:67], a[140:143], v[236:239], v[64:67]\n"
    "v_exp_f32 v51, v51\n"
    "v_mfma_f32_16x16x32_bf16 v[68:71], a[144:147], v[224:227], v[40:43]\n"
    "ds_read_b64_tr_b16 a[186:187], v3 offset:54784\n"
    "v_cvt_pk_bf16_f32 v97, v50, v51\n"
    "v_mfma_f32_16x16x32_bf16 v[68:71], a[148:151], v[228:231], v[68:71]\n"
    "v_exp_f32 v52, v52\n"
    "v_mfma_f32_16x16x32_bf16 v[68:71], a[152:155], v[232:235], v[68:71]\n"
    "ds_read_b64_tr_b16 a[188:189], v4 offset:50688\n"
    "v_mfma_f32_16x16x32_bf16 v[68:71], a[156:159], v[236:239], v[68:71]\n"
    "v_exp_f32 v53, v53\n"
    "v_mfma_f32_16x16x32_bf16 v[72:75], a[128:131], v[240:243], v[44:47]\n"
    "ds_read_b64_tr_b16 a[190:191], v4 offset:54784\n"
    "v_cvt_pk_bf16_f32 v98, v52, v53\n"
    "v_mfma_f32_16x16x32_bf16 v[72:75], a[132:135], v[244:247], v[72:75]\n"
    "v_exp_f32 v54, v54\n"
    "v_mfma_f32_16x16x32_bf16 v[72:75], a[136:139], v[248:251], v[72:75]\n"
    "ds_read_b128 a[128:131], v2 offset:0\n"
    "s_add_u32 m0, s70, 32768\n"
    "v_mfma_f32_16x16x32_bf16 v[72:75], a[140:143], v[252:255], v[72:75]\n"
    "v_exp_f32 v55, v55\n"
    "v_mfma_f32_16x16x32_bf16 v[76:79], a[144:147], v[240:243], v[44:47]\n"
    "buffer_load_dwordx4 v5, s[40:43], s69 offen offset:0 lds\n"
    "v_cvt_pk_bf16_f32 v99, v54, v55\n"
    "v_mfma_f32_16x16x32_bf16 v[76:79], a[148:151], v[244:247], v[76:79]\n"
    "v_exp_f32 v56, v56\n"
    "v_mfma_f32_16x16x32_bf16 v[76:79], a[152:155], v[248:251], v[76:79]\n"
    "ds_read_b128 a[132:135], v2 offset:512\n"
    "v_mfma_f32_16x16x32_bf16 v[76:79], a[156:159], v[252:255], v[76:79]\n"
    "v_exp_f32 v57, v57\n"
    "s_waitcnt lgkmcnt(2)\n"
    "v_mfma_f32_16x16x32_bf16 a[0:3], a[160:163], v[80:83], a[0:3]\n"
    "buffer_load_dwordx4 v6, s[40:43], s69 offen offset:1024 lds\n"
    "v_cvt_pk_bf16_f32 v100, v56, v57\n"
    "v_mfma_f32_16x16x32_bf16 a[4:7], a[164:167], v[80:83], a[4:7]\n"
    "v_exp_f32 v58, v58\n"
    "v_mfma_f32_16x16x32_bf16 a[8:11], a[168:171], v[80:83], a[8:11]\n"
    "ds_read_b128 a[136:139], v2 offset:1024\n"
    "v_mfma_f32_16x16x32_bf16 a[12:15], a[172:175], v[80:83], a[12:15]\n"
    "v_exp_f32 v59, v59\n"
    "v_mfma_f32_16x16x32_bf16 a[16:19], a[176:179], v[80:83], a[16:19]\n"
    "buffer_load_dwordx4 v7, s[40:43], s69 offen offset:2048 lds\n"
    "v_cvt_pk_bf16_f32 v101, v58, v59\n"
    "v_mfma_f32_16x16x32_bf16 a[20:23], a[180:183], v[80:83], a[20:23]\n"
    "v_exp_f32 v60, v60\n"
    "v_mfma_f32_16x16x32_bf16 a[24:27], a[184:187], v[80:83], a[24:27]\n"
    "ds_read_b128 a[140:143], v2 offset:1536\n"
    "v_mfma_f32_16x16x32_bf16 a[28:31], a[188:191], v[80:83], a[28:31]\n"
    "v_exp_f32 v61, v61\n"
    "v_mfma_f32_16x16x32_bf16 a[208:211], a[232:235], v[80:83], a[208:211]\n"
    "v_mfma_f32_16x16x32_bf16 a[32:35], a[160:163], v[84:87], a[32:35]\n"
    "buffer_load_dwordx4 v8, s[40:43], s69 offen offset:3072 lds\n"
    "v_cvt_pk_bf16_f32 v102, v60, v61\n"
    "v_mfma_f32_16x16x32_bf16 a[36:39], a[164:167], v[84:87], a[36:39]\n"
    "v_exp_f32 v62, v62\n"
    "v_mfma_f32_16x16x32_bf16 a[40:43], a[168:171], v[84:87], a[40:43]\n"
    "ds_read_b128 a[144:147], v2 offset:4096\n"
    "s_add_u32 m0, s71, 32768\n"
    "v_mfma_f32_16x16x32_bf16 a[44:47], a[172:175], v[84:87], a[44:47]\n"
    "v_exp_f32 v63, v63\n"
    "v_mfma_f32_16x16x32_bf16 a[48:51], a[176:179], v[84:87], a[48:51]\n"
    "buffer_load_dwordx4 v9, s[44:47], s69 offen offset:0 lds\n"
    "v_cvt_pk_bf16_f32 v103, v62, v63\n"
    "v_mfma_f32_16x16x32_bf16 a[52:55], a[180:183], v[84:87], a[52:55]\n"
    "v_exp_f32 v64, v64\n"
    "v_mfma_f32_16x16x32_bf16 a[56:59], a[184:187], v[84:87], a[56:59]\n"
    "ds_read_b128 a[148:151], v2 offset:4608\n"
    "v_mfma_f32_16x16x32_bf16 a[60:63], a[188:191], v[84:87], a[60:63]\n"
    "v_exp_f32 v65, v65\n"
    "v_mfma_f32_16x16x32_bf16 a[212:215], a[232:235], v[84:87], a[212:215]\n"
    "v_mfma_f32_16x16x32_bf16 a[64:67], a[160:163], v[88:91], a[64:67]\n"
    "buffer_load_dwordx4 v10, s[44:47], s69 offen offset:1024 lds\n"
    "v_cvt_pk_bf16_f32 v104, v64, v65\n"
    "v_mfma_f32_16x16x32_bf16 a[68:71], a[164:167], v[88:91], a[68:71]\n"
    "v_exp_f32 v66, v66\n"
    "v_mfma_f32_16x16x32_bf16 a[72:75], a[168:171], v[88:91], a[72:75]\n"
    "ds_read_b128 a[152:155], v2 offset:5120\n"
    "v_mfma_f32_16x16x32_bf16 a[76:79], a[172:175], v[88:91], a[76:79]\n"
    "v_exp_f32 v67, v67\n"
    "v_mfma_f32_16x16x32_bf16 a[80:83], a[176:179], v[88:91], a[80:83]\n"
    "buffer_load_dwordx4 v9, s[44:47], s69 offen offset:2048 lds\n"
    "v_cvt_pk_bf16_f32 v105, v66, v67\n"
    "v_mfma_f32_16x16x32_bf16 a[84:87], a[180:183], v[88:91], a[84:87]\n"
    "v_exp_f32 v68, v68\n"
    "v_mfma_f32_16x16x32_bf16 a[88:91], a[184:187], v[88:91], a[88:91]\n"
    "ds_read_b128 a[156:159], v2 offset:5632\n"
    "v_mfma_f32_16x16x32_bf16 a[92:95], a[188:191], v[88:91], a[92:95]\n"
    "v_exp_f32 v69, v69\n"
    "v_mfma_f32_16x16x32_bf16 a[216:219], a[232:235], v[88:91], a[216:219]\n"
    "v_mfma_f32_16x16x32_bf16 a[96:99], a[160:163], v[92:95], a[96:99]\n"
    "buffer_load_dwordx4 v10, s[44:47], s69 offen offset:3072 lds\n"
    "v_cvt_pk_bf16_f32 v106, v68, v69\n"
    "v_mfma_f32_16x16x32_bf16 a[100:103], a[164:167], v[92:95], a[100:103]\n"
    "v_exp_f32 v70, v70\n"
    "v_mfma_f32_16x16x32_bf16 a[104:107], a[168:171], v[92:95], a[104:107]\n"
    "ds_read_b64_tr_b16 a[160:161], v3 offset:57344\n"
    "v_mfma_f32_16x16x32_bf16 a[108:111], a[172:175], v[92:95], a[108:111]\n"
    "v_exp_f32 v71, v71\n"
    "v_mfma_f32_16x16x32_bf16 a[112:115], a[176:179], v[92:95], a[112:115]\n"
    "ds_read_b64_tr_b16 a[162:163], v3 offset:61440\n"
    "v_cvt_pk_bf16_f32 v107, v70, v71\n"
    "v_mfma_f32_16x16x32_bf16 a[116:119], a[180:183], v[92:95], a[116:119]\n"
    "v_exp_f32 v72, v72\n"
    "v_mfma_f32_16x16x32_bf16 a[120:123], a[184:187], v[92:95], a[120:123]\n"
    "ds_read_b64_tr_b16 a[164:165], v4 offset:57344\n"
    "v_mfma_f32_16x16x32_bf16 a[124:127], a[188:191], v[92:95], a[124:127]\n"
    "v_exp_f32 v73, v73\n"
    "v_mfma_f32_16x16x32_bf16 a[220:223], a[232:235], v[92:95], a[220:223]\n"
    "s_sub_u32 s67, s67, 1\n"
    "s_cmp_eq_u32 s67, 0\n"
    "s_cbranch_scc1 LTAIL%=\n"
    "s_waitcnt lgkmcnt(3)\n"
    "v_mfma_f32_16x16x32_bf16 v[48:51], a[128:131], v[192:195], v[32:35]\n"
    "ds_read_b64_tr_b16 a[166:167], v4 offset:61440\n"
    "v_cvt_pk_bf16_f32 v108, v72, v73\n"
    "v_mfma_f32_16x16x32_bf16 v[48:51], a[132:135], v[196:199], v[48:51]\n"
    "v_exp_f32 v74, v74\n"
    "v_mfma_f32_16x16x32_bf16 v[48:51], a[136:139], v[200:203], v[48:51]\n"
    "ds_read_b64_tr_b16 a[168:169], v3 offset:57856\n"
    "v_mfma_f32_16x16x32_bf16 v[48:51], a[140:143], v[204:207], v[48:51]\n"
    "v_exp_f32 v75, v75\n"
    "v_mfma_f32_16x16x32_bf16 v[52:55], a[144:147], v[192:195], v[32:35]\n"
    "ds_read_b64_tr_b16 a[170:171], v3 offset:61952\n"
    "v_cvt_pk_bf16_f32 v109, v74, v75\n"
    "v_mfma_f32_16x16x32_bf16 v[52:55], a[148:151], v[196:199], v[52:55]\n"
    "v_exp_f32 v76, v76\n"
    "v_mfma_f32_16x16x32_bf16 v[52:55], a[152:155], v[200:203], v[52:55]\n"
    "ds_read_b64_tr_b16 a[172:173], v4 offset:57856\n"
    "v_mfma_f32_16x16x32_bf16 v[52:55], a[156:159], v[204:207], v[52:55]\n"
    "v_exp_f32 v77, v77\n"
    "v_mfma_f32_16x16x32_bf16 v[56:59], a[128:131], v[208:211], v[36:39]\n"
    "ds_read_b64_tr_b16 a[174:175], v4 offset:61952\n"
    "v_cvt_pk_bf16_f32 v110, v76, v77\n"
    "v_mfma_f32_16x16x32_bf16 v[56:59], a[132:135], v[212:215], v[56:59]\n"
    "v_exp_f32 v78, v78\n"
    "v_mfma_f32_16x16x32_bf16 v[56:59], a[136:139], v[216:219], v[56:59]\n"
    "ds_read_b64_tr_b16 a[176:177], v3 offset:58368\n"
    "v_mfma_f32_16x16x32_bf16 v[56:59], a[140:143], v[220:223], v[56:59]\n"
    "v_exp_f32 v79, v79\n"
    "v_mfma_f32_16x16x32_bf16 v[60:63], a[144:147], v[208:211], v[36:39]\n"
    "ds_read_b64_tr_b16 a[178:179], v3 offset:62464\n"
    "v_cvt_pk_bf16_f32 v111, v78, v79\n"
    "v_mfma_f32_16x16x32_bf16 v[60:63], a[148:151], v[212:215], v[60:63]\n"
    "v_exp_f32 v48, v48\n"
    "v_mfma_f32_16x16x32_bf16 v[60:63], a[152:155], v[216:219], v[60:63]\n"
    "ds_read_b64_tr_b16 a[180:181], v4 offset:58368\n"
    "v_mfma_f32_16x16x32_bf16 v[60:63], a[156:159], v[220:223], v[60:63]\n"
    "v_exp_f32 v49, v49\n"
    "v_mfma_f32_16x16x32_bf16 v[64:67], a[128:131], v[224:227], v[40:43]\n"
    "ds_read_b64_tr_b16 a[182:183], v4 offset:62464\n"
    "v_cvt_pk_bf16_f32 v80, v48, v49\n"
    "v_mfma_f32_16x16x32_bf16 v[64:67], a[132:135], v[228:231], v[64:67]\n"
    "v_exp_f32 v50, v50\n"
    "v_mfma_f32_16x16x32_bf16 v[64:67], a[136:139], v[232:235], v[64:67]\n"
    "ds_read_b64_tr_b16 a[184:185], v3 offset:58880\n"
    "v_mfma_f32_16x16x32_bf16 v[64:67], a[140:143], v[236:239], v[64:67]\n"
    "v_exp_f32 v51, v51\n"
    "v_mfma_f32_16x16x32_bf16 v[68:71], a[144:147], v[224:227], v[40:43]\n"
    "ds_read_b64_tr_b16 a[186:187], v3 offset:62976\n"
    "v_cvt_pk_bf16_f32 v81, v50, v51\n"
    "v_mfma_f32_16x16x32_bf16 v[68:71], a[148:151], v[228:231], v[68:71]\n"
    "v_exp_f32 v52, v52\n"
    "v_mfma_f32_16x16x32_bf16 v[68:71], a[152:155], v[232:235], v[68:71]\n"
    "ds_read_b64_tr_b16 a[188:189], v4 offset:58880\n"
    "v_mfma_f32_16x16x32_bf16 v[68:71], a[156:159], v[236:239], v[68:71]\n"
    "v_exp_f32 v53, v53\n"
    "v_mfma_f32_16x16x32_bf16 v[72:75], a[128:131], v[240:243], v[44:47]\n"
    "ds_read_b64_tr_b16 a[190:191], v4 offset:62976\n"
    "v_cvt_pk_bf16_f32 v82, v52, v53\n"
    "v_mfma_f32_16x16x32_bf16 v[72:75], a[132:135], v[244:247], v[72:75]\n"
    "v_exp_f32 v54, v54\n"
    "v_mfma_f32_16x16x32_bf16 v[72:75], a[136:139], v[248:251], v[72:75]\n"
    "ds_read_b128 a[128:131], v2 offset:8192\n"
    "v_mfma_f32_16x16x32_bf16 v[72:75], a[140:143], v[252:255], v[72:75]\n"
    "v_exp_f32 v55, v55\n"
    "v_mfma_f32_16x16x32_bf16 v[76:79], a[144:147], v[240:243], v[44:47]\n"
    "v_cvt_pk_bf16_f32 v83, v54, v55\n"
    "v_mfma_f32_16x16x32_bf16 v[76:79], a[148:151], v[244:247], v[76:79]\n"
    "v_exp_f32 v56, v56\n"
    "v_mfma_f32_16x16x32_bf16 v[76:79], a[152:155], v[248:251], v[76:79]\n"
    "ds_read_b128 a[132:135], v2 offset:8704\n"
    "v_mfma_f32_16x16x32_bf16 v[76:79], a[156:159], v[252:255], v[76:79]\n"
    "v_exp_f32 v57, v57\n"
    "s_waitcnt lgkmcnt(2)\n"
    "v_mfma_f32_16x16x32_bf16 a[0:3], a[160:163], v[96:99], a[0:3]\n"
    "v_cvt_pk_bf16_f32 v84, v56, v57\n"
    "v_mfma_f32_16x16x32_bf16 a[4:7], a[164:167], v[96:99], a[4:7]\n"
    "v_exp_f32 v58, v58\n"
    "v_mfma_f32_16x16x32_bf16 a[8:11], a[168:171], v[96:99], a[8:11]\n"
    "ds_read_b128 a[136:139], v2 offset:9216\n"
    "v_mfma_f32_16x16x32_bf16 a[12:15], a[172:175], v[96:99], a[12:15]\n"
    "v_exp_f32 v59, v59\n"
    "v_mfma_f32_16x16x32_bf16 a[16:19], a[176:179], v[96:99], a[16:19]\n"
    "v_cvt_pk_bf16_f32 v85, v58, v59\n"
    "v_mfma_f32_16x16x32_bf16 a[20:23], a[180:183], v[96:99], a[20:23]\n"
    "v_exp_f32 v60, v60\n"
    "v_mfma_f32_16x16x32_bf16 a[24:27], a[184:187], v[96:99], a[24:27]\n"
    "ds_read_b128 a[140:143], v2 offset:9728\n"
    "v_mfma_f32_16x16x32_bf16 a[28:31], a[188:191], v[96:99], a[28:31]\n"
    "v_exp_f32 v61, v61\n"
    "v_mfma_f32_16x16x32_bf16 a[208:211], a[232:235], v[96:99], a[208:211]\n"
    "v_mfma_f32_16x16x32_bf16 a[32:35], a[160:163], v[100:103], a[32:35]\n"
    "v_cvt_pk_bf16_f32 v86, v60, v61\n"
    "v_mfma_f32_16x16x32_bf16 a[36:39], a[164:167], v[100:103], a[36:39]\n"
    "v_exp_f32 v62, v62\n"
    "v_mfma_f32_16x16x32_bf16 a[40:43], a[168:171], v[100:103], a[40:43]\n"
    "ds_read_b128 a[144:147], v2 offset:12288\n"
    "v_mfma_f32_16x16x32_bf16 a[44:47], a[172:175], v[100:103], a[44:47]\n"
    "v_exp_f32 v63, v63\n"
    "v_mfma_f32_16x16x32_bf16 a[48:51], a[176:179], v[100:103], a[48:51]\n"
    "v_cvt_pk_bf16_f32 v87, v62, v63\n"
    "v_mfma_f32_16x16x32_bf16 a[52:55], a[180:183], v[100:103], a[52:55]\n"
    "v_exp_f32 v64, v64\n"
    "v_mfma_f32_16x16x32_bf16 a[56:59], a[184:187], v[100:103], a[56:59]\n"
    "ds_read_b128 a[148:151], v2 offset:12800\n"
    "v_mfma_f32_16x16x32_bf16 a[60:63], a[188:191], v[100:103], a[60:63]\n"
    "v_exp_f32 v65, v65\n"
    "v_mfma_f32_16x16x32_bf16 a[212:215], a[232:235], v[100:103], a[212:215]\n"
    "v_mfma_f32_16x16x32_bf16 a[64:67], a[160:163], v[104:107], a[64:67]\n"
    "v_cvt_pk_bf16_f32 v88, v64, v65\n"
    "v_mfma_f32_16x16x32_bf16 a[68:71], a[164:167], v[104:107], a[68:71]\n"
    "v_exp_f32 v66, v66\n"
    "v_mfma_f32_16x16x32_bf16 a[72:75], a[168:171], v[104:107], a[72:75]\n"
    "ds_read_b128 a[152:155], v2 offset:13312\n"
    "v_mfma_f32_16x16x32_bf16 a[76:79], a[172:175], v[104:107], a[76:79]\n"
    "v_exp_f32 v67, v67\n"
    "v_mfma_f32_16x16x32_bf16 a[80:83], a[176:179], v[104:107], a[80:83]\n"
    "v_cvt_pk_bf16_f32 v89, v66, v67\n"
    "v_mfma_f32_16x16x32_bf16 a[84:87], a[180:183], v[104:107], a[84:87]\n"
    "v_exp_f32 v68, v68\n"
    "v_mfma_f32_16x16x32_bf16 a[88:91], a[184:187], v[104:107], a[88:91]\n"
    "ds_read_b128 a[156:159], v2 offset:13824\n"
    "v_mfma_f32_16x16x32_bf16 a[92:95], a[188:191], v[104:107], a[92:95]\n"
    "v_exp_f32 v69, v69\n"
    "v_mfma_f32_16x16x32_bf16 a[216:219], a[232:235], v[104:107], a[216:219]\n"
    "v_mfma_f32_16x16x32_bf16 a[96:99], a[160:163], v[108:111], a[96:99]\n"
    "v_cvt_pk_bf16_f32 v90, v68, v69\n"
    "s_waitcnt vmcnt(12)\n"
    "s_barrier\n"
    "v_mfma_f32_16x16x32_bf16 a[100:103], a[164:167], v[108:111], a[100:103]\n"
    "v_exp_f32 v70, v70\n"
    "v_mfma_f32_16x16x32_bf16 a[104:107], a[168:171], v[108:111], a[104:107]\n"
    "ds_read_b64_tr_b16 a[160:161], v3 offset:0\n"
    "v_mfma_f32_16x16x32_bf16 a[108:111], a[172:175], v[108:111], a[108:111]\n"
    "v_exp_f32 v71, v71\n"
    "v_mfma_f32_16x16x32_bf16 a[112:115], a[176:179], v[108:111], a[112:115]\n"
    "ds_read_b64_tr_b16 a[162:163], v3 offset:4096\n"
    "v_cvt_pk_bf16_f32 v91, v70, v71\n"
    "v_mfma_f32_16x16x32_bf16 a[116:119], a[180:183], v[108:111], a[116:119]\n"
    "v_exp_f32 v72, v72\n"
    "v_mfma_f32_16x16x32_bf16 a[120:123], a[184:187], v[108:111], a[120:123]\n"
    "ds_read_b64_tr_b16 a[164:165], v4 offset:0\n"
    "v_mfma_f32_16x16x32_bf16 a[124:127], a[188:191], v[108:111], a[124:127]\n"
    "v_exp_f32 v73, v73\n"
    "v_mfma_f32_16x16x32_bf16 a[220:223], a[232:235], v[108:111], a[220:223]\n"
    "s_branch LLOOP%=\n"
    "LTAIL%=:\n"
    "ds_read_b64_tr_b16 a[166:167], v4 offset:61440\n"
    "ds_read_b64_tr_b16 a[168:169], v3 offset:57856\n"
    "ds_read_b64_tr_b16 a[170:171], v3 offset:61952\n"
    "ds_read_b64_tr_b16 a[172:173], v4 offset:57856\n"
    "ds_read_b64_tr_b16 a[174:175], v4 offset:61952\n"
    "ds_read_b64_tr_b16 a[176:177], v3 offset:58368\n"
    "ds_read_b64_tr_b16 a[178:179], v3 offset:62464\n"
    "ds_read_b64_tr_b16 a[180:181], v4 offset:58368\n"
    "ds_read_b64_tr_b16 a[182:183], v4 offset:62464\n"
    "ds_read_b64_tr_b16 a[184:185], v3 offset:58880\n"
    "ds_read_b64_tr_b16 a[186:187], v3 offset:62976\n"
    "ds_read_b64_tr_b16 a[188:189], v4 offset:58880\n"
    "ds_read_b64_tr_b16 a[190:191], v4 offset:62976\n"
    "v_cvt_pk_bf16_f32 v108, v72, v73\n"
    "v_exp_f32 v74, v74\n"
    "v_exp_f32 v75, v75\n"
    "v_exp_f32 v76, v76\n"
    "v_exp_f32 v77, v77\n"
    "v_exp_f32 v78, v78\n"
    "v_exp_f32 v79, v79\n"
    "v_cvt_pk_bf16_f32 v109, v74, v75\n"
    "v_cvt_pk_bf16_f32 v110, v76, v77\n"
    "v_cvt_pk_bf16_f32 v111, v78, v79\n"
    "s_nop 1\n"
    "v_mfma_f32_16x16x32_bf16 a[208:211], a[232:235], v[96:99], a[208:211]\n"
    "v_mfma_f32_16x16x32_bf16 a[212:215], a[232:235], v[100:103], a[212:215]\n"
    "v_mfma_f32_16x16x32_bf16 a[216:219], a[232:235], v[104:107], a[216:219]\n"
    "v_mfma_f32_16x16x32_bf16 a[220:223], a[232:235], v[108:111], a[220:223]\n"
    "s_waitcnt lgkmcnt(0)\n"
    "v_mfma_f32_16x16x32_bf16 a[0:3], a[160:163], v[96:99], a[0:3]\n"
    "v_mfma_f32_16x16x32_bf16 a[4:7], a[164:167], v[96:99], a[4:7]\n"
    "v_mfma_f32_16x16x32_bf16 a[8:11], a[168:171], v[96:99], a[8:11]\n"
    "v_mfma_f32_16x16x32_bf16 a[12:15], a[172:175], v[96:99], a[12:15]\n"
    "v_mfma_f32_16x16x32_bf16 a[16:19], a[176:179], v[96:99], a[16:19]\n"
    "v_mfma_f32_16x16x32_bf16 a[20:23], a[180:183], v[96:99], a[20:23]\n"
    "v_mfma_f32_16x16x32_bf16 a[24:27], a[184:187], v[96:99], a[24:27]\n"
    "v_lshlrev_b32 v23, 4, v1\n"
    "v_mov_b32 v24, 0x5f800000\n"
    "v_accvgpr_read_b32 v15, a208\n"
    "v_mfma_f32_16x16x32_bf16 a[28:31], a[188:191], v[96:99], a[28:31]\n"
    "v_accvgpr_read_b32 v16, a212\n"
    "v_accvgpr_read_b32 v17, a216\n"
    "v_accvgpr_read_b32 v18, a220\n"
    "v_mfma_f32_16x16x32_bf16 a[32:35], a[160:163], v[100:103], a[32:35]\n"
    "v_rcp_f32 v26, v15\n"
    "v_rcp_f32 v28, v16\n"
    "v_rcp_f32 v30, v17\n"
    "v_mfma_f32_16x16x32_bf16 a[36:39], a[164:167], v[100:103], a[36:39]\n"
    "v_rcp_f32 v12, v18\n"
    "v_max3_f32 v25, v15, v16, v17\n"
    "v_add_f32 v27, v15, v16\n"
    "v_mfma_f32_16x16x32_bf16 a[40:43], a[168:171], v[100:103], a[40:43]\n"
    "v_add_f32 v29, v17, v18\n"
    "v_max_f32 v25, v25, v18\n"
    "v_add_f32 v27, v27, v29\n"
    "v_mfma_f32_16x16x32_bf16 a[44:47], a[172:175], v[100:103], a[44:47]\n"
    "v_cmp_nle_f32 vcc, v25, v24\n"
    "v_cmp_u_f32 s[84:85], v27, v27\n"
    "s_lshl_b32 s73, s66, 2\n"
    "v_mfma_f32_16x16x32_bf16 a[48:51], a[176:179], v[100:103], a[48:51]\n"
    "s_add_u32 s73, s73, s64\n"
    "s_mul_i32 s73, s73, 544768\n"
    "s_lshl_b32 s74, s65, 12\n"
    "v_mfma_f32_16x16x32_bf16 a[52:55], a[180:183], v[100:103], a[52:55]\n"
    "s_add_u32 s73, s73, s74\n"
    "s_add_u32 s74, s73, 136192\n"
    "s_add_u32 s75, s73, 272384\n"
    "v_mfma_f32_16x16x32_bf16 a[56:59], a[184:187], v[100:103], a[56:59]\n"
    "s_add_u32 s76, s73, 408576\n"
    "s_or_b64 vcc, vcc, s[84:85]\n"
    "v_mov_b32 v25, 1\n"
    "v_mfma_f32_16x16x32_bf16 a[60:63], a[188:191], v[100:103], a[60:63]\n"
    "v_mov_b32 v27, 147456\n"
    "v_accvgpr_read_b32 v48, a0\n"
    "v_accvgpr_read_b32 v49, a1\n"
    "v_mfma_f32_16x16x32_bf16 a[64:67], a[160:163], v[104:107], a[64:67]\n"
    "v_accvgpr_read_b32 v50, a2\n"
    "v_accvgpr_read_b32 v51, a3\n"
    "v_accvgpr_read_b32 v52, a4\n"
    "v_mfma_f32_16x16x32_bf16 a[68:71], a[164:167], v[104:107], a[68:71]\n"
    "v_accvgpr_read_b32 v53, a5\n"
    "v_accvgpr_read_b32 v54, a6\n"
    "v_accvgpr_read_b32 v55, a7\n"
    "v_mfma_f32_16x16x32_bf16 a[72:75], a[168:171], v[104:107], a[72:75]\n"
    "v_mul_f32 v48, v48, v26\n"
    "v_mul_f32 v49, v49, v26\n"
    "v_mul_f32 v50, v50, v26\n"
    "v_mfma_f32_16x16x32_bf16 a[76:79], a[172:175], v[104:107], a[76:79]\n"
    "v_mul_f32 v51, v51, v26\n"
    "v_mul_f32 v52, v52, v26\n"
    "v_mul_f32 v53, v53, v26\n"
    "v_mfma_f32_16x16x32_bf16 a[80:83], a[176:179], v[104:107], a[80:83]\n"
    "v_mul_f32 v54, v54, v26\n"
    "v_mul_f32 v55, v55, v26\n"
    "v_cvt_pk_bf16_f32 v128, v48, v49\n"
    "v_mfma_f32_16x16x32_bf16 a[84:87], a[180:183], v[104:107], a[84:87]\n"
    "v_cvt_pk_bf16_f32 v129, v50, v51\n"
    "v_cvt_pk_bf16_f32 v130, v52, v53\n"
    "v_cvt_pk_bf16_f32 v131, v54, v55\n"
    "v_mfma_f32_16x16x32_bf16 a[88:91], a[184:187], v[104:107], a[88:91]\n"
    "buffer_store_dwordx4 v[128:131], v23, s[52:55], s73 offen offset:0 sc1\n"
    "v_accvgpr_read_b32 v56, a8\n"
    "v_accvgpr_read_b32 v57, a9\n"
    "v_mfma_f32_16x16x32_bf16 a[92:95], a[188:191], v[104:107], a[92:95]\n"
    "v_accvgpr_read_b32 v58, a10\n"
    "v_accvgpr_read_b32 v59, a11\n"
    "v_accvgpr_read_b32 v60, a12\n"
    "v_mfma_f32_16x16x32_bf16 a[96:99], a[160:163], v[108:111], a[96:99]\n"
    "v_accvgpr_read_b32 v61, a13\n"
    "v_accvgpr_read_b32 v62, a14\n"
    "v_accvgpr_read_b32 v63, a15\n"
    "v_mfma_f32_16x16x32_bf16 a[100:103], a[164:167], v[108:111], a[100:103]\n"
    "v_mul_f32 v56, v56, v26\n"
    "v_mul_f32 v57, v57, v26\n"
    "v_mul_f32 v58, v58, v26\n"
    "v_mfma_f32_16x16x32_bf16 a[104:107], a[168:171], v[108:111], a[104:107]\n"
    "v_mul_f32 v59, v59, v26\n"
    "v_mul_f32 v60, v60, v26\n"
    "v_mul_f32 v61, v61, v26\n"
    "v_mfma_f32_16x16x32_bf16 a[108:111], a[172:175], v[108:111], a[108:111]\n"
    "v_mul_f32 v62, v62, v26\n"
    "v_mul_f32 v63, v63, v26\n"
    "v_cvt_pk_bf16_f32 v132, v56, v57\n"
    "v_mfma_f32_16x16x32_bf16 a[112:115], a[176:179], v[108:111], a[112:115]\n"
    "v_cvt_pk_bf16_f32 v133, v58, v59\n"
    "v_cvt_pk_bf16_f32 v134, v60, v61\n"
    "v_cvt_pk_bf16_f32 v135, v62, v63\n"
    "v_mfma_f32_16x16x32_bf16 a[116:119], a[180:183], v[108:111], a[116:119]\n"
    "buffer_store_dwordx4 v[132:135], v23, s[52:55], s73 offen offset:1024 sc1\n"
    "v_mfma_f32_16x16x32_bf16 a[120:123], a[184:187], v[108:111], a[120:123]\n"
    "v_mfma_f32_16x16x32_bf16 a[124:127], a[188:191], v[108:111], a[124:127]\n"
    "s_cbranch_vccz LNOFLAG%=\n"
    "ds_write_b32 v27, v25\n"
    "LNOFLAG%=:\n"
    "v_accvgpr_read_b32 v64, a16\n"
    "v_accvgpr_read_b32 v65, a17\n"
    "v_accvgpr_read_b32 v66, a18\n"
    "v_accvgpr_read_b32 v67, a19\n"
    "v_accvgpr_read_b32 v68, a20\n"
    "v_accvgpr_read_b32 v69, a21\n"
    "v_accvgpr_read_b32 v70, a22\n"
    "v_accvgpr_read_b32 v71, a23\n"
    "v_pk_mul_f32 v[64:65], v[64:65], v[26:27] op_sel_hi:[1,0]\n"
    "v_pk_mul_f32 v[66:67], v[66:67], v[26:27] op_sel_hi:[1,0]\n"
    "v_pk_mul_f32 v[68:69], v[68:69], v[26:27] op_sel_hi:[1,0]\n"
    "v_pk_mul_f32 v[70:71], v[70:71], v[26:27] op_sel_hi:[1,0]\n"
    "v_cvt_pk_bf16_f32 v136, v64, v65\n"
    "v_cvt_pk_bf16_f32 v137, v66, v67\n"
    "v_cvt_pk_bf16_f32 v138, v68, v69\n"
    "v_cvt_pk_bf16_f32 v139, v70, v71\n"
    "buffer_store_dwordx4 v[136:139], v23, s[52:55], s73 offen offset:2048 sc1\n"
    "v_accvgpr_read_b32 v72, a24\n"
    "v_accvgpr_read_b32 v73, a25\n"
    "v_accvgpr_read_b32 v74, a26\n"
    "v_accvgpr_read_b32 v75, a27\n"
    "v_accvgpr_read_b32 v76, a28\n"
    "v_accvgpr_read_b32 v77, a29\n"
    "v_accvgpr_read_b32 v78, a30\n"
    "v_accvgpr_read_b32 v79, a31\n"
    "v_pk_mul_f32 v[72:73], v[72:73], v[26:27] op_sel_hi:[1,0]\n"
    "v_pk_mul_f32 v[74:75], v[74:75], v[26:27] op_sel_hi:[1,0]\n"
    "v_pk_mul_f32 v[76:77], v[76:77], v[26:27] op_sel_hi:[1,0]\n"
    "v_pk_mul_f32 v[78:79], v[78:79], v[26:27] op_sel_hi:[1,0]\n"
    "v_cvt_pk_bf16_f32 v140, v72, v73\n"
    "v_cvt_pk_bf16_f32 v141, v74, v75\n"
    "v_cvt_pk_bf16_f32 v142, v76, v77\n"
    "v_cvt_pk_bf16_f32 v143, v78, v79\n"
    "buffer_store_dwordx4 v[140:143], v23, s[52:55], s73 offen offset:3072 sc1\n"
    "v_accvgpr_read_b32 v48, a32\n"
    "v_accvgpr_read_b32 v49, a33\n"
    "v_accvgpr_read_b32 v50, a34\n"
    "v_accvgpr_read_b32 v51, a35\n"
    "v_accvgpr_read_b32 v52, a36\n"
    "v_accvgpr_read_b32 v53, a37\n"
    "v_accvgpr_read_b32 v54, a38\n"
    "v_accvgpr_read_b32 v55, a39\n"
    "v_pk_mul_f32 v[48:49], v[48:49], v[28:29] op_sel_hi:[1,0]\n"
    "v_pk_mul_f32 v[50:51], v[50:51], v[28:29] op_sel_hi:[1,0]\n"
    "v_pk_mul_f32 v[52:53], v[52:53], v[28:29] op_sel_hi:[1,0]\n"
    "v_pk_mul_f32 v[54:55], v[54:55], v[28:29] op_sel_hi:[1,0]\n"
    "v_cvt_pk_bf16_f32 v128, v48, v49\n"
    "v_cvt_pk_bf16_f32 v129, v50, v51\n"
    "v_cvt_pk_bf16_f32 v130, v52, v53\n"
    "v_cvt_pk_bf16_f32 v131, v54, v55\n"
    "buffer_store_dwordx4 v[128:131], v23, s[52:55], s74 offen offset:0 sc1\n"
    "v_accvgpr_read_b32 v56, a40\n"
    "v_accvgpr_read_b32 v57, a41\n"
    "v_accvgpr_read_b32 v58, a42\n"
    "v_accvgpr_read_b32 v59, a43\n"
    "v_accvgpr_read_b32 v60, a44\n"
    "v_accvgpr_read_b32 v61, a45\n"
    "v_accvgpr_read_b32 v62, a46\n"
    "v_accvgpr_read_b32 v63, a47\n"
    "v_pk_mul_f32 v[56:57], v[56:57], v[28:29] op_sel_hi:[1,0]\n"
    "v_pk_mul_f32 v[58:59], v[58:59], v[28:29] op_sel_hi:[1,0]\n"
    "v_pk_mul_f32 v[60:61], v[60:61], v[28:29] op_sel_hi:[1,0]\n"
    "v_pk_mul_f32 v[62:63], v[62:63], v[28:29] op_sel_hi:[1,0]\n"
    "v_cvt_pk_bf16_f32 v132, v56, v57\n"
    "v_cvt_pk_bf16_f32 v133, v58, v59\n"
    "v_cvt_pk_bf16_f32 v134, v60, v61\n"
    "v_cvt_pk_bf16_f32 v135, v62, v63\n"
    "buffer_store_dwordx4 v[132:135], v23, s[52:55], s74 offen offset:1024 sc1\n"
    "v_accvgpr_read_b32 v64, a48\n"
    "v_accvgpr_read_b32 v65, a49\n"
    "v_accvgpr_read_b32 v66, a50\n"
    "v_accvgpr_read_b32 v67, a51\n"
    "v_accvgpr_read_b32 v68, a52\n"
    "v_accvgpr_read_b32 v69, a53\n"
    "v_accvgpr_read_b32 v70, a54\n"
    "v_accvgpr_read_b32 v71, a55\n"
    "v_pk_mul_f32 v[64:65], v[64:65], v[28:29] op_sel_hi:[1,0]\n"
    "v_pk_mul_f32 v[66:67], v[66:67], v[28:29] op_sel_hi:[1,0]\n"
    "v_pk_mul_f32 v[68:69], v[68:69], v[28:29] op_sel_hi:[1,0]\n"
    "v_pk_mul_f32 v[70:71], v[70:71], v[28:29] op_sel_hi:[1,0]\n"
    "v_cvt_pk_bf16_f32 v136, v64, v65\n"
    "v_cvt_pk_bf16_f32 v137, v66, v67\n"
    "v_cvt_pk_bf16_f32 v138, v68, v69\n"
    "v_cvt_pk_bf16_f32 v139, v70, v71\n"
    "buffer_store_dwordx4 v[136:139], v23, s[52:55], s74 offen offset:2048 sc1\n"
    "v_accvgpr_read_b32 v72, a56\n"
    "v_accvgpr_read_b32 v73, a57\n"
    "v_accvgpr_read_b32 v74, a58\n"
    "v_accvgpr_read_b32 v75, a59\n"
    "v_accvgpr_read_b32 v76, a60\n"
    "v_accvgpr_read_b32 v77, a61\n"
    "v_accvgpr_read_b32 v78, a62\n"
    "v_accvgpr_read_b32 v79, a63\n"
    "v_pk_mul_f32 v[72:73], v[72:73], v[28:29] op_sel_hi:[1,0]\n"
    "v_pk_mul_f32 v[74:75], v[74:75], v[28:29] op_sel_hi:[1,0]\n"
    "v_pk_mul_f32 v[76:77], v[76:77], v[28:29] op_sel_hi:[1,0]\n"
    "v_pk_mul_f32 v[78:79], v[78:79], v[28:29] op_sel_hi:[1,0]\n"
    "v_cvt_pk_bf16_f32 v140, v72, v73\n"
    "v_cvt_pk_bf16_f32 v141, v74, v75\n"
    "v_cvt_pk_bf16_f32 v142, v76, v77\n"
    "v_cvt_pk_bf16_f32 v143, v78, v79\n"
    "buffer_store_dwordx4 v[140:143], v23, s[52:55], s74 offen offset:3072 sc1\n"
    "v_accvgpr_read_b32 v48, a64\n"
    "v_accvgpr_read_b32 v49, a65\n"
    "v_accvgpr_read_b32 v50, a66\n"
    "v_accvgpr_read_b32 v51, a67\n"
    "v_accvgpr_read_b32 v52, a68\n"
    "v_accvgpr_read_b32 v53, a69\n"
    "v_accvgpr_read_b32 v54, a70\n"
    "v_accvgpr_read_b32 v55, a71\n"
    "v_pk_mul_f32 v[48:49], v[48:49], v[30:31] op_sel_hi:[1,0]\n"
    "v_pk_mul_f32 v[50:51], v[50:51], v[30:31] op_sel_hi:[1,0]\n"
    "v_pk_mul_f32 v[52:53], v[52:53], v[30:31] op_sel_hi:[1,0]\n"
    "v_pk_mul_f32 v[54:55], v[54:55], v[30:31] op_sel_hi:[1,0]\n"
    "v_cvt_pk_bf16_f32 v128, v48, v49\n"
    "v_cvt_pk_bf16_f32 v129, v50, v51\n"
    "v_cvt_pk_bf16_f32 v130, v52, v53\n"
    "v_cvt_pk_bf16_f32 v131, v54, v55\n"
    "buffer_store_dwordx4 v[128:131], v23, s[52:55], s75 offen offset:0 sc1\n"
    "v_accvgpr_read_b32 v56, a72\n"
    "v_accvgpr_read_b32 v57, a73\n"
    "v_accvgpr_read_b32 v58, a74\n"
    "v_accvgpr_read_b32 v59, a75\n"
    "v_accvgpr_read_b32 v60, a76\n"
    "v_accvgpr_read_b32 v61, a77\n"
    "v_accvgpr_read_b32 v62, a78\n"
    "v_accvgpr_read_b32 v63, a79\n"
    "v_pk_mul_f32 v[56:57], v[56:57], v[30:31] op_sel_hi:[1,0]\n"
    "v_pk_mul_f32 v[58:59], v[58:59], v[30:31] op_sel_hi:[1,0]\n"
    "v_pk_mul_f32 v[60:61], v[60:61], v[30:31] op_sel_hi:[1,0]\n"
    "v_pk_mul_f32 v[62:63], v[62:63], v[30:31] op_sel_hi:[1,0]\n"
    "v_cvt_pk_bf16_f32 v132, v56, v57\n"
    "v_cvt_pk_bf16_f32 v133, v58, v59\n"
    "v_cvt_pk_bf16_f32 v134, v60, v61\n"
    "v_cvt_pk_bf16_f32 v135, v62, v63\n"
    "buffer_store_dwordx4 v[132:135], v23, s[52:55], s75 offen offset:1024 sc1\n"
    "v_accvgpr_read_b32 v64, a80\n"
    "v_accvgpr_read_b32 v65, a81\n"
    "v_accvgpr_read_b32 v66, a82\n"
    "v_accvgpr_read_b32 v67, a83\n"
    "v_accvgpr_read_b32 v68, a84\n"
    "v_accvgpr_read_b32 v69, a85\n"
    "v_accvgpr_read_b32 v70, a86\n"
    "v_accvgpr_read_b32 v71, a87\n"
    "v_pk_mul_f32 v[64:65], v[64:65], v[30:31] op_sel_hi:[1,0]\n"
    "v_pk_mul_f32 v[66:67], v[66:67], v[30:31] op_sel_hi:[1,0]\n"
    "v_pk_mul_f32 v[68:69], v[68:69], v[30:31] op_sel_hi:[1,0]\n"
    "v_pk_mul_f32 v[70:71], v[70:71], v[30:31] op_sel_hi:[1,0]\n"
    "v_cvt_pk_bf16_f32 v136, v64, v65\n"
    "v_cvt_pk_bf16_f32 v137, v66, v67\n"
    "v_cvt_pk_bf16_f32 v138, v68, v69\n"
    "v_cvt_pk_bf16_f32 v139, v70, v71\n"
    "buffer_store_dwordx4 v[136:139], v23, s[52:55], s75 offen offset:2048 sc1\n"
    "v_accvgpr_read_b32 v72, a88\n"
    "v_accvgpr_read_b32 v73, a89\n"
    "v_accvgpr_read_b32 v74, a90\n"
    "v_accvgpr_read_b32 v75, a91\n"
    "v_accvgpr_read_b32 v76, a92\n"
    "v_accvgpr_read_b32 v77, a93\n"
    "v_accvgpr_read_b32 v78, a94\n"
    "v_accvgpr_read_b32 v79, a95\n"
    "v_pk_mul_f32 v[72:73], v[72:73], v[30:31] op_sel_hi:[1,0]\n"
    "v_pk_mul_f32 v[74:75], v[74:75], v[30:31] op_sel_hi:[1,0]\n"
    "v_pk_mul_f32 v[76:77], v[76:77], v[30:31] op_sel_hi:[1,0]\n"
    "v_pk_mul_f32 v[78:79], v[78:79], v[30:31] op_sel_hi:[1,0]\n"
    "v_cvt_pk_bf16_f32 v140, v72, v73\n"
    "v_cvt_pk_bf16_f32 v141, v74, v75\n"
    "v_cvt_pk_bf16_f32 v142, v76, v77\n"
    "v_cvt_pk_bf16_f32 v143, v78, v79\n"
    "buffer_store_dwordx4 v[140:143], v23, s[52:55], s75 offen offset:3072 sc1\n"
    "v_accvgpr_read_b32 v48, a96\n"
    "v_accvgpr_read_b32 v49, a97\n"
    "v_accvgpr_read_b32 v50, a98\n"
    "v_accvgpr_read_b32 v51, a99\n"
    "v_accvgpr_read_b32 v52, a100\n"
    "v_accvgpr_read_b32 v53, a101\n"
    "v_accvgpr_read_b32 v54, a102\n"
    "v_accvgpr_read_b32 v55, a103\n"
    "v_pk_mul_f32 v[48:49], v[48:49], v[12:13] op_sel_hi:[1,0]\n"
    "v_pk_mul_f32 v[50:51], v[50:51], v[12:13] op_sel_hi:[1,0]\n"
    "v_pk_mul_f32 v[52:53], v[52:53], v[12:13] op_sel_hi:[1,0]\n"
    "v_pk_mul_f32 v[54:55], v[54:55], v[12:13] op_sel_hi:[1,0]\n"
    "v_cvt_pk_bf16_f32 v128, v48, v49\n"
    "v_cvt_pk_bf16_f32 v129, v50, v51\n"
    "v_cvt_pk_bf16_f32 v130, v52, v53\n"
    "v_cvt_pk_bf16_f32 v131, v54, v55\n"
    "buffer_store_dwordx4 v[128:131], v23, s[52:55], s76 offen offset:0 sc1\n"
    "v_accvgpr_read_b32 v56, a104\n"
    "v_accvgpr_read_b32 v57, a105\n"
    "v_accvgpr_read_b32 v58, a106\n"
    "v_accvgpr_read_b32 v59, a107\n"
    "v_accvgpr_read_b32 v60, a108\n"
    "v_accvgpr_read_b32 v61, a109\n"
    "v_accvgpr_read_b32 v62, a110\n"
    "v_accvgpr_read_b32 v63, a111\n"
    "v_pk_mul_f32 v[56:57], v[56:57], v[12:13] op_sel_hi:[1,0]\n"
    "v_pk_mul_f32 v[58:59], v[58:59], v[12:13] op_sel_hi:[1,0]\n"
    "v_pk_mul_f32 v[60:61], v[60:61], v[12:13] op_sel_hi:[1,0]\n"
    "v_pk_mul_f32 v[62:63], v[62:63], v[12:13] op_sel_hi:[1,0]\n"
    "v_cvt_pk_bf16_f32 v132, v56, v57\n"
    "v_cvt_pk_bf16_f32 v133, v58, v59\n"
    "v_cvt_pk_bf16_f32 v134, v60, v61\n"
    "v_cvt_pk_bf16_f32 v135, v62, v63\n"
    "buffer_store_dwordx4 v[132:135], v23, s[52:55], s76 offen offset:1024 sc1\n"
    "v_accvgpr_read_b32 v64, a112\n"
    "v_accvgpr_read_b32 v65, a113\n"
    "v_accvgpr_read_b32 v66, a114\n"
    "v_accvgpr_read_b32 v67, a115\n"
    "v_accvgpr_read_b32 v68, a116\n"
    "v_accvgpr_read_b32 v69, a117\n"
    "v_accvgpr_read_b32 v70, a118\n"
    "v_accvgpr_read_b32 v71, a119\n"
    "v_pk_mul_f32 v[64:65], v[64:65], v[12:13] op_sel_hi:[1,0]\n"
    "v_pk_mul_f32 v[66:67], v[66:67], v[12:13] op_sel_hi:[1,0]\n"
    "v_pk_mul_f32 v[68:69], v[68:69], v[12:13] op_sel_hi:[1,0]\n"
    "v_pk_mul_f32 v[70:71], v[70:71], v[12:13] op_sel_hi:[1,0]\n"
    "v_cvt_pk_bf16_f32 v136, v64, v65\n"
    "v_cvt_pk_bf16_f32 v137, v66, v67\n"
    "v_cvt_pk_bf16_f32 v138, v68, v69\n"
    "v_cvt_pk_bf16_f32 v139, v70, v71\n"
    "buffer_store_dwordx4 v[136:139], v23, s[52:55], s76 offen offset:2048 sc1\n"
    "v_accvgpr_read_b32 v72, a120\n"
    "v_accvgpr_read_b32 v73, a121\n"
    "v_accvgpr_read_b32 v74, a122\n"
    "v_accvgpr_read_b32 v75, a123\n"
    "v_accvgpr_read_b32 v76, a124\n"
    "v_accvgpr_read_b32 v77, a125\n"
    "v_accvgpr_read_b32 v78, a126\n"
    "v_accvgpr_read_b32 v79, a127\n"
    "v_pk_mul_f32 v[72:73], v[72:73], v[12:13] op_sel_hi:[1,0]\n"
    "v_pk_mul_f32 v[74:75], v[74:75], v[12:13] op_sel_hi:[1,0]\n"
    "v_pk_mul_f32 v[76:77], v[76:77], v[12:13] op_sel_hi:[1,0]\n"
    "v_pk_mul_f32 v[78:79], v[78:79], v[12:13] op_sel_hi:[1,0]\n"
    "v_cvt_pk_bf16_f32 v140, v72, v73\n"
    "v_cvt_pk_bf16_f32 v141, v74, v75\n"
    "v_cvt_pk_bf16_f32 v142, v76, v77\n"
    "v_cvt_pk_bf16_f32 v143, v78, v79\n"
    "buffer_store_dwordx4 v[140:143], v23, s[52:55], s76 offen offset:3072 sc1\n"
    "s_barrier\n"
    "ds_read_b32 v24, v27\n"
    "s_waitcnt lgkmcnt(0)\n"
    "v_readfirstlane_b32 s73, v24\n"
    "s_nop 3\n"
    "s_andn2_b32 s73, s73, s72\n"
    "s_cmp_eq_u32 s73, 0\n"
    "s_cbranch_scc1 LEND%=\n"
    "s_mov_b32 s72, 1\n"
    "s_mov_b32 s42, 525312\n"
    "s_mov_b32 s46, 525312\n"
    "s_mov_b32 s69, s70\n"
    "s_mov_b32 s67, 32\n"
    "v_mov_b32 v11, 0xff800000\n"
    "v_mov_b32 v12, 0xff800000\n"
    "v_mov_b32 v13, 0xff800000\n"
    "v_mov_b32 v14, 0xff800000\n"
    "s_waitcnt vmcnt(0)\n"
    "LMAX%=:\n"
    "s_barrier\n"
    "s_add_u32 m0, s70, 0\n"
    "s_nop 0\n"
    "buffer_load_dwordx4 v5, s[40:43], s69 offen offset:0 lds\n"
    "buffer_load_dwordx4 v6, s[40:43], s69 offen offset:1024 lds\n"
    "buffer_load_dwordx4 v7, s[40:43], s69 offen offset:2048 lds\n"
    "buffer_load_dwordx4 v8, s[40:43], s69 offen offset:3072 lds\n"
    "s_waitcnt vmcnt(0)\n"
    "s_barrier\n"
    "ds_read_b128 a[128:131], v2 offset:0\n"
    "ds_read_b128 a[132:135], v2 offset:512\n"
    "ds_read_b128 a[136:139], v2 offset:1024\n"
    "ds_read_b128 a[140:143], v2 offset:1536\n"
    "ds_read_b128 a[144:147], v2 offset:4096\n"
    "ds_read_b128 a[148:151], v2 offset:4608\n"
    "ds_read_b128 a[152:155], v2 offset:5120\n"
    "ds_read_b128 a[156:159], v2 offset:5632\n"
    "s_waitcnt lgkmcnt(0)\n"
    "v_mfma_f32_16x16x32_bf16 v[48:51], a[128:131], v[192:195], 0\n"
    "v_mfma_f32_16x16x32_bf16 v[48:51], a[132:135], v[196:199], v[48:51]\n"
    "v_mfma_f32_16x16x32_bf16 v[48:51], a[136:139], v[200:203], v[48:51]\n"
    "v_mfma_f32_16x16x32_bf16 v[48:51], a[140:143], v[204:207], v[48:51]\n"
    "v_mfma_f32_16x16x32_bf16 v[52:55], a[144:147], v[192:195], 0\n"
    "v_mfma_f32_16x16x32_bf16 v[52:55], a[148:151], v[196:199], v[52:55]\n"
    "v_mfma_f32_16x16x32_bf16 v[52:55], a[152:155], v[200:203], v[52:55]\n"
    "v_mfma_f32_16x16x32_bf16 v[52:55], a[156:159], v[204:207], v[52:55]\n"
    "v_mfma_f32_16x16x32_bf16 v[56:59], a[128:131], v[208:211], 0\n"
    "v_mfma_f32_16x16x32_bf16 v[56:59], a[132:135], v[212:215], v[56:59]\n"
    "v_mfma_f32_16x16x32_bf16 v[56:59], a[136:139], v[216:219], v[56:59]\n"
    "v_mfma_f32_16x16x32_bf16 v[56:59], a[140:143], v[220:223], v[56:59]\n"
    "v_mfma_f32_16x16x32_bf16 v[60:63], a[144:147], v[208:211], 0\n"
    "v_mfma_f32_16x16x32_bf16 v[60:63], a[148:151], v[212:215], v[60:63]\n"
    "v_mfma_f32_16x16x32_bf16 v[60:63], a[152:155], v[216:219], v[60:63]\n"
    "v_mfma_f32_16x16x32_bf16 v[60:63], a[156:159], v[220:223], v[60:63]\n"
    "v_mfma_f32_16x16x32_bf16 v[64:67], a[128:131], v[224:227], 0\n"
    "v_mfma_f32_16x16x32_bf16 v[64:67], a[132:135], v[228:231], v[64:67]\n"
    "v_mfma_f32_16x16x32_bf16 v[64:67], a[136:139], v[232:235], v[64:67]\n"
    "v_mfma_f32_16x16x32_bf16 v[64:67], a[140:143], v[236:239], v[64:67]\n"
    "v_mfma_f32_16x16x32_bf16 v[68:71], a[144:147], v[224:227], 0\n"
    "v_mfma_f32_16x16x32_bf16 v[68:71], a[148:151], v[228:231], v[68:71]\n"
    "v_mfma_f32_16x16x32_bf16 v[68:71], a[152:155], v[232:235], v[68:71]\n"
    "v_mfma_f32_16x16x32_bf16 v[68:71], a[156:159], v[236:239], v[68:71]\n"
    "v_mfma_f32_16x16x32_bf16 v[72:75], a[128:131], v[240:243], 0\n"
    "v_mfma_f32_16x16x32_bf16 v[72:75], a[132:135], v[244:247], v[72:75]\n"
    "v_mfma_f32_16x16x32_bf16 v[72:75], a[136:139], v[248:251], v[72:75]\n"
    "v_mfma_f32_16x16x32_bf16 v[72:75], a[140:143], v[252:255], v[72:75]\n"
    "v_mfma_f32_16x16x32_bf16 v[76:79], a[144:147], v[240:243], 0\n"
    "v_mfma_f32_16x16x32_bf16 v[76:79], a[148:151], v[244:247], v[76:79]\n"
    "v_mfma_f32_16x16x32_bf16 v[76:79], a[152:155], v[248:251], v[76:79]\n"
    "v_mfma_f32_16x16x32_bf16 v[76:79], a[156:159], v[252:255], v[76:79]\n"
    "s_nop 7\n"
    "v_max3_f32 v11, v11, v48, v49\n"
    "v_max3_f32 v11, v11, v50, v51\n"
    "v_max3_f32 v11, v11, v52, v53\n"
    "v_max3_f32 v11, v11, v54, v55\n"
    "v_max3_f32 v12, v12, v56, v57\n"
    "v_max3_f32 v12, v12, v58, v59\n"
    "v_max3_f32 v12, v12, v60, v61\n"
    "v_max3_f32 v12, v12, v62, v63\n"
    "v_max3_f32 v13, v13, v64, v65\n"
    "v_max3_f32 v13, v13, v66, v67\n"
    "v_max3_f32 v13, v13, v68, v69\n"
    "v_max3_f32 v13, v13, v70, v71\n"
    "v_max3_f32 v14, v14, v72, v73\n"
    "v_max3_f32 v14, v14, v74, v75\n"
    "v_max3_f32 v14, v14, v76, v77\n"
    "v_max3_f32 v14, v14, v78, v79\n"
    "ds_read_b128 a[128:131], v2 offset:8192\n"
    "ds_read_b128 a[132:135], v2 offset:8704\n"
    "ds_read_b128 a[136:139], v2 offset:9216\n"
    "ds_read_b128 a[140:143], v2 offset:9728\n"
    "ds_read_b128 a[144:147], v2 offset:12288\n"
    "ds_read_b128 a[148:151], v2 offset:12800\n"
    "ds_read_b128 a[152:155], v2 offset:13312\n"
    "ds_read_b128 a[156:159], v2 offset:13824\n"
    "s_waitcnt lgkmcnt(0)\n"
    "v_mfma_f32_16x16x32_bf16 v[48:51], a[128:131], v[192:195], 0\n"
    "v_mfma_f32_16x16x32_bf16 v[48:51], a[132:135], v[196:199], v[48:51]\n"
    "v_mfma_f32_16x16x32_bf16 v[48:51], a[136:139], v[200:203], v[48:51]\n"
    "v_mfma_f32_16x16x32_bf16 v[48:51], a[140:143], v[204:207], v[48:51]\n"
    "v_mfma_f32_16x16x32_bf16 v[52:55], a[144:147], v[192:195], 0\n"
    "v_mfma_f32_16x16x32_bf16 v[52:55], a[148:151], v[196:199], v[52:55]\n"
    "v_mfma_f32_16x16x32_bf16 v[52:55], a[152:155], v[200:203], v[52:55]\n"
    "v_mfma_f32_16x16x32_bf16 v[52:55], a[156:159], v[204:207], v[52:55]\n"
    "v_mfma_f32_16x16x32_bf16 v[56:59], a[128:131], v[208:211], 0\n"
    "v_mfma_f32_16x16x32_bf16 v[56:59], a[132:135], v[212:215], v[56:59]\n"
    "v_mfma_f32_16x16x32_bf16 v[56:59], a[136:139], v[216:219], v[56:59]\n"
    "v_mfma_f32_16x16x32_bf16 v[56:59], a[140:143], v[220:223], v[56:59]\n"
    "v_mfma_f32_16x16x32_bf16 v[60:63], a[144:147], v[208:211], 0\n"
    "v_mfma_f32_16x16x32_bf16 v[60:63], a[148:151], v[212:215], v[60:63]\n"
    "v_mfma_f32_16x16x32_bf16 v[60:63], a[152:155], v[216:219], v[60:63]\n"
    "v_mfma_f32_16x16x32_bf16 v[60:63], a[156:159], v[220:223], v[60:63]\n"
    "v_mfma_f32_16x16x32_bf16 v[64:67], a[128:131], v[224:227], 0\n"
    "v_mfma_f32_16x16x32_bf16 v[64:67], a[132:135], v[228:231], v[64:67]\n"
    "v_mfma_f32_16x16x32_bf16 v[64:67], a[136:139], v[232:235], v[64:67]\n"
    "v_mfma_f32_16x16x32_bf16 v[64:67], a[140:143], v[236:239], v[64:67]\n"
    "v_mfma_f32_16x16x32_bf16 v[68:71], a[144:147], v[224:227], 0\n"
    "v_mfma_f32_16x16x32_bf16 v[68:71], a[148:151], v[228:231], v[68:71]\n"
    "v_mfma_f32_16x16x32_bf16 v[68:71], a[152:155], v[232:235], v[68:71]\n"
    "v_mfma_f32_16x16x32_bf16 v[68:71], a[156:159], v[236:239], v[68:71]\n"
    "v_mfma_f32_16x16x32_bf16 v[72:75], a[128:131], v[240:243], 0\n"
    "v_mfma_f32_16x16x32_bf16 v[72:75], a[132:135], v[244:247], v[72:75]\n"
    "v_mfma_f32_16x16x32_bf16 v[72:75], a[136:139], v[248:251], v[72:75]\n"
    "v_mfma_f32_16x16x32_bf16 v[72:75], a[140:143], v[252:255], v[72:75]\n"
    "v_mfma_f32_16x16x32_bf16 v[76:79], a[144:147], v[240:243], 0\n"
    "v_mfma_f32_16x16x32_bf16 v[76:79], a[148:151], v[244:247], v[76:79]\n"
    "v_mfma_f32_16x16x32_bf16 v[76:79], a[152:155], v[248:251], v[76:79]\n"
    "v_mfma_f32_16x16x32_bf16 v[76:79], a[156:159], v[252:255], v[76:79]\n"
    "s_nop 7\n"
    "v_max3_f32 v11, v11, v48, v49\n"
    "v_max3_f32 v11, v11, v50, v51\n"
    "v_max3_f32 v11, v11, v52, v53\n"
    "v_max3_f32 v11, v11, v54, v55\n"
    "v_max3_f32 v12, v12, v56, v57\n"
    "v_max3_f32 v12, v12, v58, v59\n"
    "v_max3_f32 v12, v12, v60, v61\n"
    "v_max3_f32 v12, v12, v62, v63\n"
    "v_max3_f32 v13, v13, v64, v65\n"
    "v_max3_f32 v13, v13, v66, v67\n"
    "v_max3_f32 v13, v13, v68, v69\n"
    "v_max3_f32 v13, v13, v70, v71\n"
    "v_max3_f32 v14, v14, v72, v73\n"
    "v_max3_f32 v14, v14, v74, v75\n"
    "v_max3_f32 v14, v14, v76, v77\n"
    "v_max3_f32 v14, v14, v78, v79\n"
    "s_add_u32 s69, s69, 16384\n"
    "s_sub_u32 s67, s67, 1\n"
    "s_cmp_lg_u32 s67, 0\n"
    "s_cbranch_scc1 LMAX%=\n"
    "v_mov_b32 v112, v11\n"
    "v_mov_b32 v113, v12\n"
    "v_mov_b32 v114, v13\n"
    "v_mov_b32 v115, v14\n"
    "s_nop 0\n"
    "v_permlane16_swap_b32 v11, v112\n"
    "v_permlane16_swap_b32 v12, v113\n"
    "v_permlane16_swap_b32 v13, v114\n"
    "v_permlane16_swap_b32 v14, v115\n"
    "v_max_f32 v11, v11, v112\n"
    "v_max_f32 v12, v12, v113\n"
    "v_max_f32 v13, v13, v114\n"
    "v_max_f32 v14, v14, v115\n"
    "v_mov_b32 v112, v11\n"
    "v_mov_b32 v113, v12\n"
    "v_mov_b32 v114, v13\n"
    "v_mov_b32 v115, v14\n"
    "s_nop 0\n"
    "v_permlane32_swap_b32 v11, v112\n"
    "v_permlane32_swap_b32 v12, v113\n"
    "v_permlane32_swap_b32 v13, v114\n"
    "v_permlane32_swap_b32 v14, v115\n"
    "v_max_f32 v11, v11, v112\n"
    "v_max_f32 v12, v12, v113\n"
    "v_max_f32 v13, v13, v114\n"
    "v_max_f32 v14, v14, v115\n"
    "s_barrier\n"
    "s_add_u32 m0, s70, 0\n"
    "s_add_u32 s74, s80, 0\n"
    "s_and_b32 s74, s74, 0x7ffff\n"
    "buffer_load_dwordx4 v5, s[40:43], s74 offen offset:0 lds\n"
    "buffer_load_dwordx4 v6, s[40:43], s74 offen offset:1024 lds\n"
    "buffer_load_dwordx4 v7, s[40:43], s74 offen offset:2048 lds\n"
    "buffer_load_dwordx4 v8, s[40:43], s74 offen offset:3072 lds\n"
    "s_add_u32 m0, s71, 0\n"
    "s_add_u32 s74, s80, 0\n"
    "s_and_b32 s74, s74, 0x7ffff\n"
    "buffer_load_dwordx4 v9, s[44:47], s74 offen offset:0 lds\n"
    "buffer_load_dwordx4 v10, s[44:47], s74 offen offset:1024 lds\n"
    "buffer_load_dwordx4 v9, s[44:47], s74 offen offset:2048 lds\n"
    "buffer_load_dwordx4 v10, s[44:47], s74 offen offset:3072 lds\n"
    "s_add_u32 m0, s70, 16384\n"
    "s_add_u32 s74, s80, 16384\n"
    "s_and_b32 s74, s74, 0x7ffff\n"
    "buffer_load_dwordx4 v5, s[40:43], s74 offen offset:0 lds\n"
    "buffer_load_dwordx4 v6, s[40:43], s74 offen offset:1024 lds\n"
    "buffer_load_dwordx4 v7, s[40:43], s74 offen offset:2048 lds\n"
    "buffer_load_dwordx4 v8, s[40:43], s74 offen offset:3072 lds\n"
    "s_add_u32 m0, s71, 16384\n"
    "s_add_u32 s74, s80, 16384\n"
    "s_and_b32 s74, s74, 0x7ffff\n"
    "buffer_load_dwordx4 v9, s[44:47], s74 offen offset:0 lds\n"
    "buffer_load_dwordx4 v10, s[44:47], s74 offen offset:1024 lds\n"
    "buffer_load_dwordx4 v9, s[44:47], s74 offen offset:2048 lds\n"
    "buffer_load_dwordx4 v10, s[44:47], s74 offen offset:3072 lds\n"
    "s_add_u32 m0, s70, 32768\n"
    "s_add_u32 s74, s80, 32768\n"
    "s_and_b32 s74, s74, 0x7ffff\n"
    "buffer_load_dwordx4 v5, s[40:43], s74 offen offset:0 lds\n"
    "buffer_load_dwordx4 v6, s[40:43], s74 offen offset:1024 lds\n"
    "buffer_load_dwordx4 v7, s[40:43], s74 offen offset:2048 lds\n"
    "buffer_load_dwordx4 v8, s[40:43], s74 offen offset:3072 lds\n"
    "s_add_u32 m0, s71, 32768\n"
    "s_add_u32 s74, s80, 32768\n"
    "s_and_b32 s74, s74, 0x7ffff\n"
    "buffer_load_dwordx4 v9, s[44:47], s74 offen offset:0 lds\n"
    "buffer_load_dwordx4 v10, s[44:47], s74 offen offset:1024 lds\n"
    "buffer_load_dwordx4 v9, s[44:47], s74 offen offset:2048 lds\n"
    "buffer_load_dwordx4 v10, s[44:47], s74 offen offset:3072 lds\n"
    "v_accvgpr_write_b32 a0, 0\n"
    "v_accvgpr_write_b32 a1, 0\n"
    "v_accvgpr_write_b32 a2, 0\n"
    "v_accvgpr_write_b32 a3, 0\n"
    "v_accvgpr_write_b32 a4, 0\n"
    "v_accvgpr_write_b32 a5, 0\n"
    "v_accvgpr_write_b32 a6, 0\n"
    "v_accvgpr_write_b32 a7, 0\n"
    "v_accvgpr_write_b32 a8, 0\n"
    "v_accvgpr_write_b32 a9, 0\n"
    "v_accvgpr_write_b32 a10, 0\n"
    "v_accvgpr_write_b32 a11, 0\n"
    "v_accvgpr_write_b32 a12, 0\n"
    "v_accvgpr_write_b32 a13, 0\n"
    "v_accvgpr_write_b32 a14, 0\n"
    "v_accvgpr_write_b32 a15, 0\n"
    "v_accvgpr_write_b32 a16, 0\n"
    "v_accvgpr_write_b32 a17, 0\n"
    "v_accvgpr_write_b32 a18, 0\n"
    "v_accvgpr_write_b32 a19, 0\n"
    "v_accvgpr_write_b32 a20, 0\n"
    "v_accvgpr_write_b32 a21, 0\n"
    "v_accvgpr_write_b32 a22, 0\n"
    "v_accvgpr_write_b32 a23, 0\n"
    "v_accvgpr_write_b32 a24, 0\n"
    "v_accvgpr_write_b32 a25, 0\n"
    "v_accvgpr_write_b32 a26, 0\n"
    "v_accvgpr_write_b32 a27, 0\n"
    "v_accvgpr_write_b32 a28, 0\n"
    "v_accvgpr_write_b32 a29, 0\n"
    "v_accvgpr_write_b32 a30, 0\n"
    "v_accvgpr_write_b32 a31, 0\n"
    "v_accvgpr_write_b32 a32, 0\n"
    "v_accvgpr_write_b32 a33, 0\n"
    "v_accvgpr_write_b32 a34, 0\n"
    "v_accvgpr_write_b32 a35, 0\n"
    "v_accvgpr_write_b32 a36, 0\n"
    "v_accvgpr_write_b32 a37, 0\n"
    "v_accvgpr_write_b32 a38, 0\n"
    "v_accvgpr_write_b32 a39, 0\n"
    "v_accvgpr_write_b32 a40, 0\n"
    "v_accvgpr_write_b32 a41, 0\n"
    "v_accvgpr_write_b32 a42, 0\n"
    "v_accvgpr_write_b32 a43, 0\n"
    "v_accvgpr_write_b32 a44, 0\n"
    "v_accvgpr_write_b32 a45, 0\n"
    "v_accvgpr_write_b32 a46, 0\n"
    "v_accvgpr_write_b32 a47, 0\n"
    "v_accvgpr_write_b32 a48, 0\n"
    "v_accvgpr_write_b32 a49, 0\n"
    "v_accvgpr_write_b32 a50, 0\n"
    "v_accvgpr_write_b32 a51, 0\n"
    "v_accvgpr_write_b32 a52, 0\n"
    "v_accvgpr_write_b32 a53, 0\n"
    "v_accvgpr_write_b32 a54, 0\n"
    "v_accvgpr_write_b32 a55, 0\n"
    "v_accvgpr_write_b32 a56, 0\n"
    "v_accvgpr_write_b32 a57, 0\n"
    "v_accvgpr_write_b32 a58, 0\n"
    "v_accvgpr_write_b32 a59, 0\n"
    "v_accvgpr_write_b32 a60, 0\n"
    "v_accvgpr_write_b32 a61, 0\n"
    "v_accvgpr_write_b32 a62, 0\n"
    "v_accvgpr_write_b32 a63, 0\n"
    "v_accvgpr_write_b32 a64, 0\n"
    "v_accvgpr_write_b32 a65, 0\n"
    "v_accvgpr_write_b32 a66, 0\n"
    "v_accvgpr_write_b32 a67, 0\n"
    "v_accvgpr_write_b32 a68, 0\n"
    "v_accvgpr_write_b32 a69, 0\n"
    "v_accvgpr_write_b32 a70, 0\n"
    "v_accvgpr_write_b32 a71, 0\n"
    "v_accvgpr_write_b32 a72, 0\n"
    "v_accvgpr_write_b32 a73, 0\n"
    "v_accvgpr_write_b32 a74, 0\n"
    "v_accvgpr_write_b32 a75, 0\n"
    "v_accvgpr_write_b32 a76, 0\n"
    "v_accvgpr_write_b32 a77, 0\n"
    "v_accvgpr_write_b32 a78, 0\n"
    "v_accvgpr_write_b32 a79, 0\n"
    "v_accvgpr_write_b32 a80, 0\n"
    "v_accvgpr_write_b32 a81, 0\n"
    "v_accvgpr_write_b32 a82, 0\n"
    "v_accvgpr_write_b32 a83, 0\n"
    "v_accvgpr_write_b32 a84, 0\n"
    "v_accvgpr_write_b32 a85, 0\n"
    "v_accvgpr_write_b32 a86, 0\n"
    "v_accvgpr_write_b32 a87, 0\n"
    "v_accvgpr_write_b32 a88, 0\n"
    "v_accvgpr_write_b32 a89, 0\n"
    "v_accvgpr_write_b32 a90, 0\n"
    "v_accvgpr_write_b32 a91, 0\n"
    "v_accvgpr_write_b32 a92, 0\n"
    "v_accvgpr_write_b32 a93, 0\n"
    "v_accvgpr_write_b32 a94, 0\n"
    "v_accvgpr_write_b32 a95, 0\n"
    "v_accvgpr_write_b32 a96, 0\n"
    "v_accvgpr_write_b32 a97, 0\n"
    "v_accvgpr_write_b32 a98, 0\n"
    "v_accvgpr_write_b32 a99, 0\n"
    "v_accvgpr_write_b32 a100, 0\n"
    "v_accvgpr_write_b32 a101, 0\n"
    "v_accvgpr_write_b32 a102, 0\n"
    "v_accvgpr_write_b32 a103, 0\n"
    "v_accvgpr_write_b32 a104, 0\n"
    "v_accvgpr_write_b32 a105, 0\n"
    "v_accvgpr_write_b32 a106, 0\n"
    "v_accvgpr_write_b32 a107, 0\n"
    "v_accvgpr_write_b32 a108, 0\n"
    "v_accvgpr_write_b32 a109, 0\n"
    "v_accvgpr_write_b32 a110, 0\n"
    "v_accvgpr_write_b32 a111, 0\n"
    "v_accvgpr_write_b32 a112, 0\n"
    "v_accvgpr_write_b32 a113, 0\n"
    "v_accvgpr_write_b32 a114, 0\n"
    "v_accvgpr_write_b32 a115, 0\n"
    "v_accvgpr_write_b32 a116, 0\n"
    "v_accvgpr_write_b32 a117, 0\n"
    "v_accvgpr_write_b32 a118, 0\n"
    "v_accvgpr_write_b32 a119, 0\n"
    "v_accvgpr_write_b32 a120, 0\n"
    "v_accvgpr_write_b32 a121, 0\n"
    "v_accvgpr_write_b32 a122, 0\n"
    "v_accvgpr_write_b32 a123, 0\n"
    "v_accvgpr_write_b32 a124, 0\n"
    "v_accvgpr_write_b32 a125, 0\n"
    "v_accvgpr_write_b32 a126, 0\n"
    "v_accvgpr_write_b32 a127, 0\n"
    "s_waitcnt vmcnt(0)\n"
    "s_barrier\n"
    "ds_read_b128 a[128:131], v2 offset:0\n"
    "ds_read_b128 a[132:135], v2 offset:512\n"
    "ds_read_b128 a[136:139], v2 offset:1024\n"
    "ds_read_b128 a[140:143], v2 offset:1536\n"
    "ds_read_b128 a[144:147], v2 offset:4096\n"
    "ds_read_b128 a[148:151], v2 offset:4608\n"
    "ds_read_b128 a[152:155], v2 offset:5120\n"
    "ds_read_b128 a[156:159], v2 offset:5632\n"
    "s_branch LSTART%=\n"
    "LEND%=:\n"
    :: "s"(x), "s"(kc), "s"(vc), "s"(wq), "s"(wo), "s"(wr), "s"(ws), "s"(blockIdx.x), "v"(threadIdx.x), "s"((unsigned)(size_t)lds), "s"(wg), "s"(wu)
    : "memory", "vcc", "scc", "s31", "s32", "s33", "s34", "s35", "s36", "s37", "s38", "s39", "s40", "s41", "s42", "s43", "s44", "s45", "s46", "s47", "s48", "s49", "s50", "s51", "s52", "s53", "s54", "s55", "s56", "s57", "s58", "s59", "s60", "s61", "s62", "s63", "s64", "s65", "s66", "s67", "s68", "s69", "s70", "s71", "s72", "s73", "s74", "s75", "s76", "s77", "s78", "s79", "s80", "s81", "s82", "s83", "s84", "s85", "s86", "s87", "s88", "s89", "s90", "s91", "s92", "s93", "s94", "s95", "s96", "s97", "s98", "s99", "s100", "s101", "v1", "v2", "v3", "v4", "v5", "v6", "v7", "v8", "v9", "v10", "v11", "v12", "v13", "v14", "v15", "v16", "v17", "v18", "v19", "v20", "v21", "v22", "v23", "v24", "v25", "v26", "v27", "v28", "v29", "v30", "v31", "v32", "v33", "v34", "v35", "v36", "v37", "v38", "v39", "v40", "v41", "v42", "v43", "v44", "v45", "v46", "v47", "v48", "v49", "v50", "v51", "v52", "v53", "v54", "v55", "v56", "v57", "v58", "v59", "v60", "v61", "v62", "v63", "v64", "v65", "v66", "v67", "v68", "v69", "v70", "v71", "v72", "v73", "v74", "v75", "v76", "v77", "v78", "v79", "v80", "v81", "v82", "v83", "v84", "v85", "v86", "v87", "v88", "v89", "v90", "v91", "v92", "v93", "v94", "v95", "v96", "v97", "v98", "v99", "v100", "v101", "v102", "v103", "v104", "v105", "v106", "v107", "v108", "v109", "v110", "v111", "v112", "v113", "v114", "v115", "v116", "v117", "v118", "v119", "v120", "v121", "v122", "v123", "v124", "v125", "v126", "v127", "v128", "v129", "v130", "v131", "v132", "v133", "v134", "v135", "v136", "v137", "v138", "v139", "v140", "v141", "v142", "v143", "v144", "v145", "v146", "v147", "v148", "v149", "v150", "v151", "v152", "v153", "v154", "v155", "v156", "v157", "v158", "v159", "v160", "v161", "v162", "v163", "v164", "v165", "v166", "v167", "v168", "v169", "v170", "v171", "v172", "v173", "v174", "v175", "v176", "v177", "v178", "v179", "v180", "v181", "v182", "v183", "v184", "v185", "v186", "v187", "v188", "v189", "v190", "v191", "v192", "v193", "v194", "v195", "v196", "v197", "v198", "v199", "v200", "v201", "v202", "v203", "v204", "v205", "v206", "v207", "v208", "v209", "v210", "v211", "v212", "v213", "v214", "v215", "v216", "v217", "v218", "v219", "v220", "v221", "v222", "v223", "v224", "v225", "v226", "v227", "v228", "v229", "v230", "v231", "v232", "v233", "v234", "v235", "v236", "v237", "v238", "v239", "v240", "v241", "v242", "v243", "v244", "v245", "v246", "v247", "v248", "v249", "v250", "v251", "v252", "v253", "v254", "v255", "a0", "a1", "a2", "a3", "a4", "a5", "a6", "a7", "a8", "a9", "a10", "a11", "a12", "a13", "a14", "a15", "a16", "a17", "a18", "a19", "a20", "a21", "a22", "a23", "a24", "a25", "a26", "a27", "a28", "a29", "a30", "a31", "a32", "a33", "a34", "a35", "a36", "a37", "a38", "a39", "a40", "a41", "a42", "a43", "a44", "a45", "a46", "a47", "a48", "a49", "a50", "a51", "a52", "a53", "a54", "a55", "a56", "a57", "a58", "a59", "a60", "a61", "a62", "a63", "a64", "a65", "a66", "a67", "a68", "a69", "a70", "a71", "a72", "a73", "a74", "a75", "a76", "a77", "a78", "a79", "a80", "a81", "a82", "a83", "a84", "a85", "a86", "a87", "a88", "a89", "a90", "a91", "a92", "a93", "a94", "a95", "a96", "a97", "a98", "a99", "a100", "a101", "a102", "a103", "a104", "a105", "a106", "a107", "a108", "a109", "a110", "a111", "a112", "a113", "a114", "a115", "a116", "a117", "a118", "a119", "a120", "a121", "a122", "a123", "a124", "a125", "a126", "a127", "a128", "a129", "a130", "a131", "a132", "a133", "a134", "a135", "a136", "a137", "a138", "a139", "a140", "a141", "a142", "a143", "a144", "a145", "a146", "a147", "a148", "a149", "a150", "a151", "a152", "a153", "a154", "a155", "a156", "a157", "a158", "a159", "a160", "a161", "a162", "a163", "a164", "a165", "a166", "a167", "a168", "a169", "a170", "a171", "a172", "a173", "a174", "a175", "a176", "a177", "a178", "a179", "a180", "a181", "a182", "a183", "a184", "a185", "a186", "a187", "a188", "a189", "a190", "a191", "a192", "a193", "a194", "a195", "a196", "a197", "a198", "a199", "a200", "a201", "a202", "a203", "a204", "a205", "a206", "a207", "a208", "a209", "a210", "a211", "a212", "a213", "a214", "a215", "a216", "a217", "a218", "a219", "a220", "a221", "a222", "a223", "a224", "a225", "a226", "a227", "a228", "a229", "a230", "a231", "a232", "a233", "a234", "a235", "a236", "a237", "a238", "a239", "a240", "a241", "a242", "a243", "a244", "a245", "a246", "a247", "a248", "a249", "a250", "a251", "a252", "a253", "a254", "a255");
}

DI float bfr(float v) { f32x2 t = {v, v}; return __uint_as_float(__builtin_bit_cast(unsigned, __builtin_convertvector(t, bf16x2)) << 16); }
DI unsigned pk(float a, float b) { f32x2 t = {a, b}; return __builtin_bit_cast(unsigned, __builtin_convertvector(t, bf16x2)); }
DI float blo(unsigned w) { return __uint_as_float(w << 16); }
DI float bhi(unsigned w) { return __uint_as_float(w & 0xffff0000u); }
DI float lnv(float x, float m, float rd) { return bfr(bfr(x - m) * rd); }
DI float sigm(float z) { return __builtin_amdgcn_rcpf(1.f + __builtin_amdgcn_exp2f(-1.44269504f * z)); }
template <int C> DI float dppf(float v) { return __int_as_float(__builtin_amdgcn_update_dpp(0, __float_as_int(v), C, 0xf, 0xf, true)); }
DI float rsum16(float v) { v += dppf<0x128>(v); v += dppf<0x124>(v); v += dppf<0x122>(v); v += dppf<0x121>(v); return v; }
DI float swp32(float a, float b) { auto r = __builtin_amdgcn_permlane32_swap(__float_as_uint(a), __float_as_uint(b), false, false); return __uint_as_float(r[0]) + __uint_as_float(r[1]); }
DI float swp16(float a, float b) { auto r = __builtin_amdgcn_permlane16_swap(__float_as_uint(a), __float_as_uint(b), false, false); return __uint_as_float(r[0]) + __uint_as_float(r[1]); }

DI int argmax_r(const f32x4 (&r)[8], int lane, float& wb) {
  float best = -INFINITY; int idx = 0;
#pragma unroll
  for (int i = 0; i < 8; ++i)
#pragma unroll
    for (int j = 0; j < 4; ++j) if (r[i][j] > best) { best = r[i][j]; idx = 256 * i + 4 * lane + j; }
  wb = best;
  wb = fmaxf(wb, dppf<0x128>(wb)); wb = fmaxf(wb, dppf<0x124>(wb)); wb = fmaxf(wb, dppf<0x122>(wb)); wb = fmaxf(wb, dppf<0x121>(wb));
  wb = fmaxf(fmaxf(__int_as_float(__builtin_amdgcn_readlane(__float_as_int(wb), 0)), __int_as_float(__builtin_amdgcn_readlane(__float_as_int(wb), 16))),
             fmaxf(__int_as_float(__builtin_amdgcn_readlane(__float_as_int(wb), 32)), __int_as_float(__builtin_amdgcn_readlane(__float_as_int(wb), 48))));
  return __builtin_amdgcn_readlane(idx, __builtin_ctzll(__ballot(best == wb) | (1ull << 63)));
}

#define BAR(n) { __builtin_amdgcn_sched_barrier(0); _Pragma("unroll") for (int q_ = 0; q_ < (n); ++q_) __builtin_amdgcn_s_barrier(); __builtin_amdgcn_sched_barrier(0); }
__global__ __launch_bounds__(448, 2) __attribute__((amdgpu_num_vgpr(192))) void k_out(const unsigned short* __restrict__ x, const unsigned short* __restrict__ wd, char* __restrict__ ws, unsigned short* __restrict__ out) {
  __shared__ __attribute__((aligned(16))) char lds[163840];
  const int tid = threadIdx.x, lane = tid & 63, wave = __builtin_amdgcn_readfirstlane(tid >> 6), b = blockIdx.x;
  const f32x2* stats = (const f32x2*)(ws + WS_M);
  const int ks = b & 3, rt = 8 * ((b >> 2) & 1) + ((b >> 3) & 7), ct = b >> 6, tile = 4 * rt + ct;
  float* ysh = (float*)(lds + 147456);
  int* idxsh = (int*)(lds + 147456 + 256);
  {
    asm volatile("s_getpc_b64 s[40:41]\n\ts_mov_b32 s42, 19968\n\ts_mov_b32 s43, 0x20000\n\ts_mov_b32 m0, %1\n\ts_nop 0\n\tbuffer_load_dwordx4 %0, s[40:43], 0 offen lds"
                 :: "v"(16 * tid + 7168 * (b >> 3)), "s"((unsigned)(size_t)lds + 149504 + 1024 * wave) : "s40", "s41", "s42", "s43", "memory");
  }
  if (wave == 4 || wave == 5) {
    const unsigned vo = 16 * lane;
    asm volatile(
    "s_mov_b64 s[40:41], %0\n"
    "s_mov_b32 s42, 0x1100000\n"
    "s_mov_b32 s43, 0x20000\n"
    "s_add_u32 m0, %1, 0\n"
    "s_mov_b32 s44, 0\n"
    "buffer_load_dwordx4 %2, s[40:43], s44 offen offset:0 lds\n"
    "buffer_load_dwordx4 %3, s[40:43], s44 offen offset:1024 lds\n"
    "buffer_load_dwordx4 %4, s[40:43], s44 offen offset:2048 lds\n"
    "buffer_load_dwordx4 %5, s[40:43], s44 offen offset:3072 lds\n"
    "s_add_u32 m0, %1, 4096\n"
    "s_mov_b32 s44, 544768\n"
    "buffer_load_dwordx4 %2, s[40:43], s44 offen offset:0 lds\n"
    "buffer_load_dwordx4 %3, s[40:43], s44 offen offset:1024 lds\n"
    "buffer_load_dwordx4 %4, s[40:43], s44 offen offset:2048 lds\n"
    "buffer_load_dwordx4 %5, s[40:43], s44 offen offset:3072 lds\n"
    "s_add_u32 m0, %1, 16384\n"
    "s_mov_b32 s44, 1024\n"
    "buffer_load_dwordx4 %2, s[40:43], s44 offen offset:0 lds\n"
    "buffer_load_dwordx4 %3, s[40:43], s44 offen offset:1024 lds\n"
    "buffer_load_dwordx4 %4, s[40:43], s44 offen offset:2048 lds\n"
    "buffer_load_dwordx4 %5, s[40:43], s44 offen offset:3072 lds\n"
    "s_add_u32 m0, %1, 20480\n"
    "s_mov_b32 s44, 545792\n"
    "buffer_load_dwordx4 %2, s[40:43], s44 offen offset:0 lds\n"
    "buffer_load_dwordx4 %3, s[40:43], s44 offen offset:1024 lds\n"
    "buffer_load_dwordx4 %4, s[40:43], s44 offen offset:2048 lds\n"
    "buffer_load_dwordx4 %5, s[40:43], s44 offen offset:3072 lds\n"
    "s_add_u32 m0, %1, 32768\n"
    "s_mov_b32 s44, 2048\n"
    "buffer_load_dwordx4 %2, s[40:43], s44 offen offset:0 lds\n"
    "buffer_load_dwordx4 %3, s[40:43], s44 offen offset:1024 lds\n"
    "buffer_load_dwordx4 %4, s[40:43], s44 offen offset:2048 lds\n"
    "buffer_load_dwordx4 %5, s[40:43], s44 offen offset:3072 lds\n"
    "s_add_u32 m0, %1, 36864\n"
    "s_mov_b32 s44, 546816\n"
    "buffer_load_dwordx4 %2, s[40:43], s44 offen offset:0 lds\n"
    "buffer_load_dwordx4 %3, s[40:43], s44 offen offset:1024 lds\n"
    "buffer_load_dwordx4 %4, s[40:43], s44 offen offset:2048 lds\n"
    "buffer_load_dwordx4 %5, s[40:43], s44 offen offset:3072 lds\n"
    "s_waitcnt vmcnt(16)\n"
    "s_barrier\n"
    "s_add_u32 m0, %1, 49152\n"
    "s_mov_b32 s44, 3072\n"
    "buffer_load_dwordx4 %2, s[40:43], s44 offen offset:0 lds\n"
    "buffer_load_dwordx4 %3, s[40:43], s44 offen offset:1024 lds\n"
    "buffer_load_dwordx4 %4, s[40:43], s44 offen offset:2048 lds\n"
    "buffer_load_dwordx4 %5, s[40:43], s44 offen offset:3072 lds\n"
    "s_add_u32 m0, %1, 53248\n"
    "s_mov_b32 s44, 547840\n"
    "buffer_load_dwordx4 %2, s[40:43], s44 offen offset:0 lds\n"
    "buffer_load_dwordx4 %3, s[40:43], s44 offen offset:1024 lds\n"
    "buffer_load_dwordx4 %4, s[40:43], s44 offen offset:2048 lds\n"
    "buffer_load_dwordx4 %5, s[40:43], s44 offen offset:3072 lds\n"
    "s_waitcnt vmcnt(16)\n"
    "s_barrier\n"
    "s_add_u32 m0, %1, 65536\n"
    "s_mov_b32 s44, 4096\n"
    "buffer_load_dwordx4 %2, s[40:43], s44 offen offset:0 lds\n"
    "buffer_load_dwordx4 %3, s[40:43], s44 offen offset:1024 lds\n"
    "buffer_load_dwordx4 %4, s[40:43], s44 offen offset:2048 lds\n"
    "buffer_load_dwordx4 %5, s[40:43], s44 offen offset:3072 lds\n"
    "s_add_u32 m0, %1, 69632\n"
    "s_mov_b32 s44, 548864\n"
    "buffer_load_dwordx4 %2, s[40:43], s44 offen offset:0 lds\n"
    "buffer_load_dwordx4 %3, s[40:43], s44 offen offset:1024 lds\n"
    "buffer_load_dwordx4 %4, s[40:43], s44 offen offset:2048 lds\n"
    "buffer_load_dwordx4 %5, s[40:43], s44 offen offset:3072 lds\n"
    "s_add_u32 m0, %1, 81920\n"
    "s_mov_b32 s44, 5120\n"
    "buffer_load_dwordx4 %2, s[40:43], s44 offen offset:0 lds\n"
    "buffer_load_dwordx4 %3, s[40:43], s44 offen offset:1024 lds\n"
    "buffer_load_dwordx4 %4, s[40:43], s44 offen offset:2048 lds\n"
    "buffer_load_dwordx4 %5, s[40:43], s44 offen offset:3072 lds\n"
    "s_add_u32 m0, %1, 86016\n"
    "s_mov_b32 s44, 549888\n"
    "buffer_load_dwordx4 %2, s[40:43], s44 offen offset:0 lds\n"
    "buffer_load_dwordx4 %3, s[40:43], s44 offen offset:1024 lds\n"
    "buffer_load_dwordx4 %4, s[40:43], s44 offen offset:2048 lds\n"
    "buffer_load_dwordx4 %5, s[40:43], s44 offen offset:3072 lds\n"
    "s_waitcnt vmcnt(24)\n"
    "s_barrier\n"
    "s_add_u32 m0, %1, 98304\n"
    "s_mov_b32 s44, 6144\n"
    "buffer_load_dwordx4 %2, s[40:43], s44 offen offset:0 lds\n"
    "buffer_load_dwordx4 %3, s[40:43], s44 offen offset:1024 lds\n"
    "buffer_load_dwordx4 %4, s[40:43], s44 offen offset:2048 lds\n"
    "buffer_load_dwordx4 %5, s[40:43], s44 offen offset:3072 lds\n"
    "s_add_u32 m0, %1, 102400\n"
    "s_mov_b32 s44, 550912\n"
    "buffer_load_dwordx4 %2, s[40:43], s44 offen offset:0 lds\n"
    "buffer_load_dwordx4 %3, s[40:43], s44 offen offset:1024 lds\n"
    "buffer_load_dwordx4 %4, s[40:43], s44 offen offset:2048 lds\n"
    "buffer_load_dwordx4 %5, s[40:43], s44 offen offset:3072 lds\n"
    "s_add_u32 m0, %1, 114688\n"
    "s_mov_b32 s44, 7168\n"
    "buffer_load_dwordx4 %2, s[40:43], s44 offen offset:0 lds\n"
    "buffer_load_dwordx4 %3, s[40:43], s44 offen offset:1024 lds\n"
    "buffer_load_dwordx4 %4, s[40:43], s44 offen offset:2048 lds\n"
    "buffer_load_dwordx4 %5, s[40:43], s44 offen offset:3072 lds\n"
    "s_add_u32 m0, %1, 118784\n"
    "s_mov_b32 s44, 551936\n"
    "buffer_load_dwordx4 %2, s[40:43], s44 offen offset:0 lds\n"
    "buffer_load_dwordx4 %3, s[40:43], s44 offen offset:1024 lds\n"
    "buffer_load_dwordx4 %4, s[40:43], s44 offen offset:2048 lds\n"
    "buffer_load_dwordx4 %5, s[40:43], s44 offen offset:3072 lds\n"
    "s_waitcnt vmcnt(32)\n"
    "s_barrier\n"
    "s_add_u32 m0, %1, 0\n"
    "s_mov_b32 s44, 8192\n"
    "buffer_load_dwordx4 %2, s[40:43], s44 offen offset:0 lds\n"
    "buffer_load_dwordx4 %3, s[40:43], s44 offen offset:1024 lds\n"
    "buffer_load_dwordx4 %4, s[40:43], s44 offen offset:2048 lds\n"
    "buffer_load_dwordx4 %5, s[40:43], s44 offen offset:3072 lds\n"
    "s_add_u32 m0, %1, 4096\n"
    "s_mov_b32 s44, 552960\n"
    "buffer_load_dwordx4 %2, s[40:43], s44 offen offset:0 lds\n"
    "buffer_load_dwordx4 %3, s[40:43], s44 offen offset:1024 lds\n"
    "buffer_load_dwordx4 %4, s[40:43], s44 offen offset:2048 lds\n"
    "buffer_load_dwordx4 %5, s[40:43], s44 offen offset:3072 lds\n"
    "s_waitcnt vmcnt(32)\n"
    "s_barrier\n"
    "s_add_u32 m0, %1, 16384\n"
    "s_mov_b32 s44, 9216\n"
    "buffer_load_dwordx4 %2, s[40:43], s44 offen offset:0 lds\n"
    "buffer_load_dwordx4 %3, s[40:43], s44 offen offset:1024 lds\n"
    "buffer_load_dwordx4 %4, s[40:43], s44 offen offset:2048 lds\n"
    "buffer_load_dwordx4 %5, s[40:43], s44 offen offset:3072 lds\n"
    "s_add_u32 m0, %1, 20480\n"
    "s_mov_b32 s44, 553984\n"
    "buffer_load_dwordx4 %2, s[40:43], s44 offen offset:0 lds\n"
    "buffer_load_dwordx4 %3, s[40:43], s44 offen offset:1024 lds\n"
    "buffer_load_dwordx4 %4, s[40:43], s44 offen offset:2048 lds\n"
    "buffer_load_dwordx4 %5, s[40:43], s44 offen offset:3072 lds\n"
    "s_waitcnt vmcnt(32)\n"
    "s_barrier\n"
    "s_add_u32 m0, %1, 32768\n"
    "s_mov_b32 s44, 10240\n"
    "buffer_load_dwordx4 %2, s[40:43], s44 offen offset:0 lds\n"
    "buffer_load_dwordx4 %3, s[40:43], s44 offen offset:1024 lds\n"
    "buffer_load_dwordx4 %4, s[40:43], s44 offen offset:2048 lds\n"
    "buffer_load_dwordx4 %5, s[40:43], s44 offen offset:3072 lds\n"
    "s_add_u32 m0, %1, 36864\n"
    "s_mov_b32 s44, 555008\n"
    "buffer_load_dwordx4 %2, s[40:43], s44 offen offset:0 lds\n"
    "buffer_load_dwordx4 %3, s[40:43], s44 offen offset:1024 lds\n"
    "buffer_load_dwordx4 %4, s[40:43], s44 offen offset:2048 lds\n"
    "buffer_load_dwordx4 %5, s[40:43], s44 offen offset:3072 lds\n"
    "s_waitcnt vmcnt(32)\n"
    "s_barrier\n"
    "s_add_u32 m0, %1, 49152\n"
    "s_mov_b32 s44, 11264\n"
    "buffer_load_dwordx4 %2, s[40:43], s44 offen offset:0 lds\n"
    "buffer_load_dwordx4 %3, s[40:43], s44 offen offset:1024 lds\n"
    "buffer_load_dwordx4 %4, s[40:43], s44 offen offset:2048 lds\n"
    "buffer_load_dwordx4 %5, s[40:43], s44 offen offset:3072 lds\n"
    "s_add_u32 m0, %1, 53248\n"
    "s_mov_b32 s44, 556032\n"
    "buffer_load_dwordx4 %2, s[40:43], s44 offen offset:0 lds\n"
    "buffer_load_dwordx4 %3, s[40:43], s44 offen offset:1024 lds\n"
    "buffer_load_dwordx4 %4, s[40:43], s44 offen offset:2048 lds\n"
    "buffer_load_dwordx4 %5, s[40:43], s44 offen offset:3072 lds\n"
    "s_waitcnt vmcnt(32)\n"
    "s_barrier\n"
    "s_add_u32 m0, %1, 65536\n"
    "s_mov_b32 s44, 12288\n"
    "buffer_load_dwordx4 %2, s[40:43], s44 offen offset:0 lds\n"
    "buffer_load_dwordx4 %3, s[40:43], s44 offen offset:1024 lds\n"
    "buffer_load_dwordx4 %4, s[40:43], s44 offen offset:2048 lds\n"
    "buffer_load_dwordx4 %5, s[40:43], s44 offen offset:3072 lds\n"
    "s_add_u32 m0, %1, 69632\n"
    "s_mov_b32 s44, 557056\n"
    "buffer_load_dwordx4 %2, s[40:43], s44 offen offset:0 lds\n"
    "buffer_load_dwordx4 %3, s[40:43], s44 offen offset:1024 lds\n"
    "buffer_load_dwordx4 %4, s[40:43], s44 offen offset:2048 lds\n"
    "buffer_load_dwordx4 %5, s[40:43], s44 offen offset:3072 lds\n"
    "s_waitcnt vmcnt(32)\n"
    "s_barrier\n"
    "s_add_u32 m0, %1, 81920\n"
    "s_mov_b32 s44, 13312\n"
    "buffer_load_dwordx4 %2, s[40:43], s44 offen offset:0 lds\n"
    "buffer_load_dwordx4 %3, s[40:43], s44 offen offset:1024 lds\n"
    "buffer_load_dwordx4 %4, s[40:43], s44 offen offset:2048 lds\n"
    "buffer_load_dwordx4 %5, s[40:43], s44 offen offset:3072 lds\n"
    "s_add_u32 m0, %1, 86016\n"
    "s_mov_b32 s44, 558080\n"
    "buffer_load_dwordx4 %2, s[40:43], s44 offen offset:0 lds\n"
    "buffer_load_dwordx4 %3, s[40:43], s44 offen offset:1024 lds\n"
    "buffer_load_dwordx4 %4, s[40:43], s44 offen offset:2048 lds\n"
    "buffer_load_dwordx4 %5, s[40:43], s44 offen offset:3072 lds\n"
    "s_waitcnt vmcnt(32)\n"
    "s_barrier\n"
    "s_add_u32 m0, %1, 98304\n"
    "s_mov_b32 s44, 14336\n"
    "buffer_load_dwordx4 %2, s[40:43], s44 offen offset:0 lds\n"
    "buffer_load_dwordx4 %3, s[40:43], s44 offen offset:1024 lds\n"
    "buffer_load_dwordx4 %4, s[40:43], s44 offen offset:2048 lds\n"
    "buffer_load_dwordx4 %5, s[40:43], s44 offen offset:3072 lds\n"
    "s_add_u32 m0, %1, 102400\n"
    "s_mov_b32 s44, 559104\n"
    "buffer_load_dwordx4 %2, s[40:43], s44 offen offset:0 lds\n"
    "buffer_load_dwordx4 %3, s[40:43], s44 offen offset:1024 lds\n"
    "buffer_load_dwordx4 %4, s[40:43], s44 offen offset:2048 lds\n"
    "buffer_load_dwordx4 %5, s[40:43], s44 offen offset:3072 lds\n"
    "s_waitcnt vmcnt(32)\n"
    "s_barrier\n"
    "s_add_u32 m0, %1, 114688\n"
    "s_mov_b32 s44, 15360\n"
    "buffer_load_dwordx4 %2, s[40:43], s44 offen offset:0 lds\n"
    "buffer_load_dwordx4 %3, s[40:43], s44 offen offset:1024 lds\n"
    "buffer_load_dwordx4 %4, s[40:43], s44 offen offset:2048 lds\n"
    "buffer_load_dwordx4 %5, s[40:43], s44 offen offset:3072 lds\n"
    "s_add_u32 m0, %1, 118784\n"
    "s_mov_b32 s44, 560128\n"
    "buffer_load_dwordx4 %2, s[40:43], s44 offen offset:0 lds\n"
    "buffer_load_dwordx4 %3, s[40:43], s44 offen offset:1024 lds\n"
    "buffer_load_dwordx4 %4, s[40:43], s44 offen offset:2048 lds\n"
    "buffer_load_dwordx4 %5, s[40:43], s44 offen offset:3072 lds\n"
    "s_waitcnt vmcnt(32)\n"
    "s_barrier\n"
    "s_add_u32 m0, %1, 0\n"
    "s_mov_b32 s44, 16384\n"
    "buffer_load_dwordx4 %2, s[40:43], s44 offen offset:0 lds\n"
    "buffer_load_dwordx4 %3, s[40:43], s44 offen offset:1024 lds\n"
    "buffer_load_dwordx4 %4, s[40:43], s44 offen offset:2048 lds\n"
    "buffer_load_dwordx4 %5, s[40:43], s44 offen offset:3072 lds\n"
    "s_add_u32 m0, %1, 4096\n"
    "s_mov_b32 s44, 561152\n"
    "buffer_load_dwordx4 %2, s[40:43], s44 offen offset:0 lds\n"
    "buffer_load_dwordx4 %3, s[40:43], s44 offen offset:1024 lds\n"
    "buffer_load_dwordx4 %4, s[40:43], s44 offen offset:2048 lds\n"
    "buffer_load_dwordx4 %5, s[40:43], s44 offen offset:3072 lds\n"
    "s_waitcnt vmcnt(32)\n"
    "s_barrier\n"
    "s_add_u32 m0, %1, 16384\n"
    "s_mov_b32 s44, 17408\n"
    "buffer_load_dwordx4 %2, s[40:43], s44 offen offset:0 lds\n"
    "buffer_load_dwordx4 %3, s[40:43], s44 offen offset:1024 lds\n"
    "buffer_load_dwordx4 %4, s[40:43], s44 offen offset:2048 lds\n"
    "buffer_load_dwordx4 %5, s[40:43], s44 offen offset:3072 lds\n"
    "s_add_u32 m0, %1, 20480\n"
    "s_mov_b32 s44, 562176\n"
    "buffer_load_dwordx4 %2, s[40:43], s44 offen offset:0 lds\n"
    "buffer_load_dwordx4 %3, s[40:43], s44 offen offset:1024 lds\n"
    "buffer_load_dwordx4 %4, s[40:43], s44 offen offset:2048 lds\n"
    "buffer_load_dwordx4 %5, s[40:43], s44 offen offset:3072 lds\n"
    "s_waitcnt vmcnt(32)\n"
    "s_barrier\n"
    "s_add_u32 m0, %1, 32768\n"
    "s_mov_b32 s44, 18432\n"
    "buffer_load_dwordx4 %2, s[40:43], s44 offen offset:0 lds\n"
    "buffer_load_dwordx4 %3, s[40:43], s44 offen offset:1024 lds\n"
    "buffer_load_dwordx4 %4, s[40:43], s44 offen offset:2048 lds\n"
    "buffer_load_dwordx4 %5, s[40:43], s44 offen offset:3072 lds\n"
    "s_add_u32 m0, %1, 36864\n"
    "s_mov_b32 s44, 563200\n"
    "buffer_load_dwordx4 %2, s[40:43], s44 offen offset:0 lds\n"
    "buffer_load_dwordx4 %3, s[40:43], s44 offen offset:1024 lds\n"
    "buffer_load_dwordx4 %4, s[40:43], s44 offen offset:2048 lds\n"
    "buffer_load_dwordx4 %5, s[40:43], s44 offen offset:3072 lds\n"
    "s_waitcnt vmcnt(32)\n"
    "s_barrier\n"
    "s_add_u32 m0, %1, 49152\n"
    "s_mov_b32 s44, 19456\n"
    "buffer_load_dwordx4 %2, s[40:43], s44 offen offset:0 lds\n"
    "buffer_load_dwordx4 %3, s[40:43], s44 offen offset:1024 lds\n"
    "buffer_load_dwordx4 %4, s[40:43], s44 offen offset:2048 lds\n"
    "buffer_load_dwordx4 %5, s[40:43], s44 offen offset:3072 lds\n"
    "s_add_u32 m0, %1, 53248\n"
    "s_mov_b32 s44, 564224\n"
    "buffer_load_dwordx4 %2, s[40:43], s44 offen offset:0 lds\n"
    "buffer_load_dwordx4 %3, s[40:43], s44 offen offset:1024 lds\n"
    "buffer_load_dwordx4 %4, s[40:43], s44 offen offset:2048 lds\n"
    "buffer_load_dwordx4 %5, s[40:43], s44 offen offset:3072 lds\n"
    "s_waitcnt vmcnt(32)\n"
    "s_barrier\n"
    "s_add_u32 m0, %1, 65536\n"
    "s_mov_b32 s44, 20480\n"
    "buffer_load_dwordx4 %2, s[40:43], s44 offen offset:0 lds\n"
    "buffer_load_dwordx4 %3, s[40:43], s44 offen offset:1024 lds\n"
    "buffer_load_dwordx4 %4, s[40:43], s44 offen offset:2048 lds\n"
    "buffer_load_dwordx4 %5, s[40:43], s44 offen offset:3072 lds\n"
    "s_add_u32 m0, %1, 69632\n"
    "s_mov_b32 s44, 565248\n"
    "buffer_load_dwordx4 %2, s[40:43], s44 offen offset:0 lds\n"
    "buffer_load_dwordx4 %3, s[40:43], s44 offen offset:1024 lds\n"
    "buffer_load_dwordx4 %4, s[40:43], s44 offen offset:2048 lds\n"
    "buffer_load_dwordx4 %5, s[40:43], s44 offen offset:3072 lds\n"
    "s_waitcnt vmcnt(32)\n"
    "s_barrier\n"
    "s_add_u32 m0, %1, 81920\n"
    "s_mov_b32 s44, 21504\n"
    "buffer_load_dwordx4 %2, s[40:43], s44 offen offset:0 lds\n"
    "buffer_load_dwordx4 %3, s[40:43], s44 offen offset:1024 lds\n"
    "buffer_load_dwordx4 %4, s[40:43], s44 offen offset:2048 lds\n"
    "buffer_load_dwordx4 %5, s[40:43], s44 offen offset:3072 lds\n"
    "s_add_u32 m0, %1, 86016\n"
    "s_mov_b32 s44, 566272\n"
    "buffer_load_dwordx4 %2, s[40:43], s44 offen offset:0 lds\n"
    "buffer_load_dwordx4 %3, s[40:43], s44 offen offset:1024 lds\n"
    "buffer_load_dwordx4 %4, s[40:43], s44 offen offset:2048 lds\n"
    "buffer_load_dwordx4 %5, s[40:43], s44 offen offset:3072 lds\n"
    "s_waitcnt vmcnt(32)\n"
    "s_barrier\n"
    "s_add_u32 m0, %1, 98304\n"
    "s_mov_b32 s44, 22528\n"
    "buffer_load_dwordx4 %2, s[40:43], s44 offen offset:0 lds\n"
    "buffer_load_dwordx4 %3, s[40:43], s44 offen offset:1024 lds\n"
    "buffer_load_dwordx4 %4, s[40:43], s44 offen offset:2048 lds\n"
    "buffer_load_dwordx4 %5, s[40:43], s44 offen offset:3072 lds\n"
    "s_add_u32 m0, %1, 102400\n"
    "s_mov_b32 s44, 567296\n"
    "buffer_load_dwordx4 %2, s[40:43], s44 offen offset:0 lds\n"
    "buffer_load_dwordx4 %3, s[40:43], s44 offen offset:1024 lds\n"
    "buffer_load_dwordx4 %4, s[40:43], s44 offen offset:2048 lds\n"
    "buffer_load_dwordx4 %5, s[40:43], s44 offen offset:3072 lds\n"
    "s_waitcnt vmcnt(32)\n"
    "s_barrier\n"
    "s_add_u32 m0, %1, 114688\n"
    "s_mov_b32 s44, 23552\n"
    "buffer_load_dwordx4 %2, s[40:43], s44 offen offset:0 lds\n"
    "buffer_load_dwordx4 %3, s[40:43], s44 offen offset:1024 lds\n"
    "buffer_load_dwordx4 %4, s[40:43], s44 offen offset:2048 lds\n"
    "buffer_load_dwordx4 %5, s[40:43], s44 offen offset:3072 lds\n"
    "s_add_u32 m0, %1, 118784\n"
    "s_mov_b32 s44, 568320\n"
    "buffer_load_dwordx4 %2, s[40:43], s44 offen offset:0 lds\n"
    "buffer_load_dwordx4 %3, s[40:43], s44 offen offset:1024 lds\n"
    "buffer_load_dwordx4 %4, s[40:43], s44 offen offset:2048 lds\n"
    "buffer_load_dwordx4 %5, s[40:43], s44 offen offset:3072 lds\n"
    "s_waitcnt vmcnt(32)\n"
    "s_barrier\n"
    "s_add_u32 m0, %1, 0\n"
    "s_mov_b32 s44, 24576\n"
    "buffer_load_dwordx4 %2, s[40:43], s44 offen offset:0 lds\n"
    "buffer_load_dwordx4 %3, s[40:43], s44 offen offset:1024 lds\n"
    "buffer_load_dwordx4 %4, s[40:43], s44 offen offset:2048 lds\n"
    "buffer_load_dwordx4 %5, s[40:43], s44 offen offset:3072 lds\n"
    "s_add_u32 m0, %1, 4096\n"
    "s_mov_b32 s44, 569344\n"
    "buffer_load_dwordx4 %2, s[40:43], s44 offen offset:0 lds\n"
    "buffer_load_dwordx4 %3, s[40:43], s44 offen offset:1024 lds\n"
    "buffer_load_dwordx4 %4, s[40:43], s44 offen offset:2048 lds\n"
    "buffer_load_dwordx4 %5, s[40:43], s44 offen offset:3072 lds\n"
    "s_waitcnt vmcnt(32)\n"
    "s_barrier\n"
    "s_add_u32 m0, %1, 16384\n"
    "s_mov_b32 s44, 25600\n"
    "buffer_load_dwordx4 %2, s[40:43], s44 offen offset:0 lds\n"
    "buffer_load_dwordx4 %3, s[40:43], s44 offen offset:1024 lds\n"
    "buffer_load_dwordx4 %4, s[40:43], s44 offen offset:2048 lds\n"
    "buffer_load_dwordx4 %5, s[40:43], s44 offen offset:3072 lds\n"
    "s_add_u32 m0, %1, 20480\n"
    "s_mov_b32 s44, 570368\n"
    "buffer_load_dwordx4 %2, s[40:43], s44 offen offset:0 lds\n"
    "buffer_load_dwordx4 %3, s[40:43], s44 offen offset:1024 lds\n"
    "buffer_load_dwordx4 %4, s[40:43], s44 offen offset:2048 lds\n"
    "buffer_load_dwordx4 %5, s[40:43], s44 offen offset:3072 lds\n"
    "s_waitcnt vmcnt(32)\n"
    "s_barrier\n"
    "s_add_u32 m0, %1, 32768\n"
    "s_mov_b32 s44, 26624\n"
    "buffer_load_dwordx4 %2, s[40:43], s44 offen offset:0 lds\n"
    "buffer_load_dwordx4 %3, s[40:43], s44 offen offset:1024 lds\n"
    "buffer_load_dwordx4 %4, s[40:43], s44 offen offset:2048 lds\n"
    "buffer_load_dwordx4 %5, s[40:43], s44 offen offset:3072 lds\n"
    "s_add_u32 m0, %1, 36864\n"
    "s_mov_b32 s44, 571392\n"
    "buffer_load_dwordx4 %2, s[40:43], s44 offen offset:0 lds\n"
    "buffer_load_dwordx4 %3, s[40:43], s44 offen offset:1024 lds\n"
    "buffer_load_dwordx4 %4, s[40:43], s44 offen offset:2048 lds\n"
    "buffer_load_dwordx4 %5, s[40:43], s44 offen offset:3072 lds\n"
    "s_waitcnt vmcnt(32)\n"
    "s_barrier\n"
    "s_add_u32 m0, %1, 49152\n"
    "s_mov_b32 s44, 27648\n"
    "buffer_load_dwordx4 %2, s[40:43], s44 offen offset:0 lds\n"
    "buffer_load_dwordx4 %3, s[40:43], s44 offen offset:1024 lds\n"
    "buffer_load_dwordx4 %4, s[40:43], s44 offen offset:2048 lds\n"
    "buffer_load_dwordx4 %5, s[40:43], s44 offen offset:3072 lds\n"
    "s_add_u32 m0, %1, 53248\n"
    "s_mov_b32 s44, 572416\n"
    "buffer_load_dwordx4 %2, s[40:43], s44 offen offset:0 lds\n"
    "buffer_load_dwordx4 %3, s[40:43], s44 offen offset:1024 lds\n"
    "buffer_load_dwordx4 %4, s[40:43], s44 offen offset:2048 lds\n"
    "buffer_load_dwordx4 %5, s[40:43], s44 offen offset:3072 lds\n"
    "s_waitcnt vmcnt(32)\n"
    "s_barrier\n"
    "s_add_u32 m0, %1, 65536\n"
    "s_mov_b32 s44, 28672\n"
    "buffer_load_dwordx4 %2, s[40:43], s44 offen offset:0 lds\n"
    "buffer_load_dwordx4 %3, s[40:43], s44 offen offset:1024 lds\n"
    "buffer_load_dwordx4 %4, s[40:43], s44 offen offset:2048 lds\n"
    "buffer_load_dwordx4 %5, s[40:43], s44 offen offset:3072 lds\n"
    "s_add_u32 m0, %1, 69632\n"
    "s_mov_b32 s44, 573440\n"
    "buffer_load_dwordx4 %2, s[40:43], s44 offen offset:0 lds\n"
    "buffer_load_dwordx4 %3, s[40:43], s44 offen offset:1024 lds\n"
    "buffer_load_dwordx4 %4, s[40:43], s44 offen offset:2048 lds\n"
    "buffer_load_dwordx4 %5, s[40:43], s44 offen offset:3072 lds\n"
    "s_waitcnt vmcnt(32)\n"
    "s_barrier\n"
    "s_add_u32 m0, %1, 81920\n"
    "s_mov_b32 s44, 29696\n"
    "buffer_load_dwordx4 %2, s[40:43], s44 offen offset:0 lds\n"
    "buffer_load_dwordx4 %3, s[40:43], s44 offen offset:1024 lds\n"
    "buffer_load_dwordx4 %4, s[40:43], s44 offen offset:2048 lds\n"
    "buffer_load_dwordx4 %5, s[40:43], s44 offen offset:3072 lds\n"
    "s_add_u32 m0, %1, 86016\n"
    "s_mov_b32 s44, 574464\n"
    "buffer_load_dwordx4 %2, s[40:43], s44 offen offset:0 lds\n"
    "buffer_load_dwordx4 %3, s[40:43], s44 offen offset:1024 lds\n"
    "buffer_load_dwordx4 %4, s[40:43], s44 offen offset:2048 lds\n"
    "buffer_load_dwordx4 %5, s[40:43], s44 offen offset:3072 lds\n"
    "s_waitcnt vmcnt(32)\n"
    "s_barrier\n"
    "s_add_u32 m0, %1, 98304\n"
    "s_mov_b32 s44, 30720\n"
    "buffer_load_dwordx4 %2, s[40:43], s44 offen offset:0 lds\n"
    "buffer_load_dwordx4 %3, s[40:43], s44 offen offset:1024 lds\n"
    "buffer_load_dwordx4 %4, s[40:43], s44 offen offset:2048 lds\n"
    "buffer_load_dwordx4 %5, s[40:43], s44 offen offset:3072 lds\n"
    "s_add_u32 m0, %1, 102400\n"
    "s_mov_b32 s44, 575488\n"
    "buffer_load_dwordx4 %2, s[40:43], s44 offen offset:0 lds\n"
    "buffer_load_dwordx4 %3, s[40:43], s44 offen offset:1024 lds\n"
    "buffer_load_dwordx4 %4, s[40:43], s44 offen offset:2048 lds\n"
    "buffer_load_dwordx4 %5, s[40:43], s44 offen offset:3072 lds\n"
    "s_waitcnt vmcnt(32)\n"
    "s_barrier\n"
    "s_add_u32 m0, %1, 114688\n"
    "s_mov_b32 s44, 31744\n"
    "buffer_load_dwordx4 %2, s[40:43], s44 offen offset:0 lds\n"
    "buffer_load_dwordx4 %3, s[40:43], s44 offen offset:1024 lds\n"
    "buffer_load_dwordx4 %4, s[40:43], s44 offen offset:2048 lds\n"
    "buffer_load_dwordx4 %5, s[40:43], s44 offen offset:3072 lds\n"
    "s_add_u32 m0, %1, 118784\n"
    "s_mov_b32 s44, 576512\n"
    "buffer_load_dwordx4 %2, s[40:43], s44 offen offset:0 lds\n"
    "buffer_load_dwordx4 %3, s[40:43], s44 offen offset:1024 lds\n"
    "buffer_load_dwordx4 %4, s[40:43], s44 offen offset:2048 lds\n"
    "buffer_load_dwordx4 %5, s[40:43], s44 offen offset:3072 lds\n"
    "s_waitcnt vmcnt(32)\n"
    "s_barrier\n"
    "s_waitcnt vmcnt(24)\n"
    "s_barrier\n"
    "s_waitcnt vmcnt(16)\n"
    "s_barrier\n"
    "s_waitcnt vmcnt(8)\n"
    "s_barrier\n"
    "s_waitcnt vmcnt(0)\n"
    "s_barrier\n"
    "s_barrier\n"
      :: "s"(wave == 4 ? ws + (size_t)(8 * rt) * FS + ks * 32768 : ws + WS_WF + (size_t)(8 * ct) * FS + ks * 32768), "s"((unsigned)(size_t)lds + 8192 * (wave - 4)),
         "v"(vo), "v"(vo + FS - 1024), "v"(vo + 2 * FS - 2048), "v"(vo + 3 * FS - 3072)
      : "memory", "scc", "s40", "s41", "s42", "s43", "s44");
    return;
  }
  if (wave == 6) {
    const int hb = b < NHELP ? b : 0;
    __builtin_amdgcn_s_setprio(3);
    f32x4 r[8];
    u32x4 g8[8], u8[8], d8[8];
#pragma unroll
    for (int i = 0; i < 8; ++i) r[i] = *(const f32x4*)(ws + WS_R + 1024 * i + 16 * lane);
    __builtin_amdgcn_sched_barrier(0);
#pragma unroll
    for (int i = 0; i < 8; ++i) {
      g8[i] = *(const u32x4*)(ws + WS_G + 16384 * hb + 1024 * i + 16 * lane);
      u8[i] = *(const u32x4*)(ws + WS_G + 16384 * hb + 8192 + 1024 * i + 16 * lane);
    }
    __builtin_amdgcn_sched_barrier(0);
#pragma unroll
    for (int i = 0; i < 8; ++i) d8[i] = *(const u32x4*)(wd + (8 * hb + i) * 512 + 8 * lane);
    __builtin_amdgcn_sched_barrier(0);
    BAR(1)
    float wb;
    const int idx = argmax_r(r, lane, wb);
    if (lane == 0) *idxsh = idx;
    if (b >= NHELP) {
      asm volatile("s_waitcnt lgkmcnt(0)" ::: "memory");
      BAR(11)
    } else {
      unsigned xs[8];
#pragma unroll
      for (int i = 0; i < 8; ++i) xs[i] = x[idx * 512 + 64 * i + lane];
      const f32x2 st = stats[idx];
      BAR(6)
      const float rd = __builtin_amdgcn_rcpf(st[1]);
      float ga[8] = {}, ua[8] = {};
#pragma unroll
      for (int i = 0; i < 8; ++i) {
        const float xv = lnv(blo(xs[i]), st[0], rd);
#pragma unroll
        for (int j = 0; j < 4; ++j) {
          ga[2 * j] += xv * blo(g8[i][j]); ga[2 * j + 1] += xv * bhi(g8[i][j]);
          ua[2 * j] += xv * blo(u8[i][j]); ua[2 * j + 1] += xv * bhi(u8[i][j]);
        }
        if (i & 1) BAR(1)
      }
      float hh[2];
#pragma unroll
      for (int j = 0; j < 2; ++j) {
        const float gg = bfr(rsum16(swp16(swp32(ga[j], ga[j + 4]), swp32(ga[j + 2], ga[j + 6]))));
        const float uu = bfr(rsum16(swp16(swp32(ua[j], ua[j + 4]), swp32(ua[j + 2], ua[j + 6]))));
        hh[j] = bfr(bfr(gg * sigm(gg)) * uu);
      }
      BAR(1)
      float y[8] = {};
#pragma unroll
      for (int j = 0; j < 8; ++j) {
        const float hj = __int_as_float(__builtin_amdgcn_readlane(__float_as_int(hh[j & 1]), 16 * (j >> 1)));
#pragma unroll
        for (int c = 0; c < 4; ++c) { y[2 * c] += hj * blo(d8[j][c]); y[2 * c + 1] += hj * bhi(d8[j][c]); }
      }
      u32x4 y0, y1;
#pragma unroll
      for (int c = 0; c < 4; ++c) { y0[c] = min(__float_as_uint(y[c]), 0xfffffffeu); y1[c] = min(__float_as_uint(y[c + 4]), 0xfffffffeu); }
      char* yp = ws + WS_Y + 2048 * hb + 32 * lane;
      asm volatile("global_store_dwordx4 %0, %1, off sc1\n\tglobal_store_dwordx4 %0, %2, off offset:16 sc1\n\ts_nop 1" :: "v"(yp), "v"(y0), "v"(y1) : "memory");
    }
    if ((idx >> 6) == 2 * rt + (ks >> 1)) {
      const __amdgpu_buffer_rsrc_t ry = __builtin_amdgcn_make_buffer_rsrc(ws + WS_Y, 0, 2048 * NHELP, 0x20000);
      const int yo = 2048 * (lane >> 4) + 4 * (128 * ct + 64 * (ks & 1)) + 16 * (lane & 15);
      BAR(12)
      f32x4 ya;
      for (int it = 0; it < (1 << 18); ++it) {
        u32x4 v[NHELP / 4];
#pragma unroll
        for (int i = 0; i < NHELP / 4; ++i) v[i] = __builtin_amdgcn_raw_buffer_load_b128(ry, yo, 8192 * i, 16);
        if (it == 0) BAR(9)
        ya = f32x4{0.f, 0.f, 0.f, 0.f};
        unsigned mx = 0;
#pragma unroll
        for (int i = 0; i < NHELP / 4; ++i) {
          mx = max(max(mx, max(v[i][0], v[i][1])), max(v[i][2], v[i][3]));
          ya += __builtin_bit_cast(f32x4, v[i]);
        }
        if (!__any(mx == 0xffffffffu)) break;
        asm volatile("" ::: "memory");
      }
      const float rs = bfr(sigm(bfr(wb)));
#pragma unroll
      for (int j = 0; j < 4; ++j) { float t = swp32(ya[j], ya[j]); t = swp16(t, t); if (lane < 16) ysh[4 * lane + j] = bfr(bfr(t) * rs); }
      __syncthreads();
    }
    return;
  }
  f32x4 acc[16] = {};
  {
    const unsigned lb = (unsigned)(size_t)lds, ra = lb + 16 * lane + 4096 * (wave >> 1), rw = lb + 16 * lane + 8192 + 4096 * (wave & 1);
    asm volatile(
    "s_mov_b32 s40, 4\n"
    "s_barrier\n"
    "ds_read_b128 a[0:3], %16 offset:0\n"
    "ds_read_b128 a[4:7], %16 offset:1024\n"
    "ds_read_b128 a[8:11], %16 offset:2048\n"
    "ds_read_b128 a[12:15], %16 offset:3072\n"
    "ds_read_b128 a[16:19], %17 offset:0\n"
    "ds_read_b128 a[20:23], %17 offset:1024\n"
    "ds_read_b128 a[24:27], %17 offset:2048\n"
    "ds_read_b128 a[28:31], %17 offset:3072\n"
    "LG%=:\n"
    "s_waitcnt lgkmcnt(0)\n"
    "s_barrier\n"
    "v_mfma_f32_16x16x32_bf16 %0, a[16:19], a[0:3], %0\n"
    "ds_read_b128 a[32:35], %16 offset:16384\n"
    "v_mfma_f32_16x16x32_bf16 %1, a[16:19], a[4:7], %1\n"
    "ds_read_b128 a[36:39], %16 offset:17408\n"
    "v_mfma_f32_16x16x32_bf16 %2, a[16:19], a[8:11], %2\n"
    "ds_read_b128 a[40:43], %16 offset:18432\n"
    "v_mfma_f32_16x16x32_bf16 %3, a[16:19], a[12:15], %3\n"
    "ds_read_b128 a[44:47], %16 offset:19456\n"
    "v_mfma_f32_16x16x32_bf16 %4, a[20:23], a[0:3], %4\n"
    "ds_read_b128 a[48:51], %17 offset:16384\n"
    "v_mfma_f32_16x16x32_bf16 %5, a[20:23], a[4:7], %5\n"
    "ds_read_b128 a[52:55], %17 offset:17408\n"
    "v_mfma_f32_16x16x32_bf16 %6, a[20:23], a[8:11], %6\n"
    "ds_read_b128 a[56:59], %17 offset:18432\n"
    "v_mfma_f32_16x16x32_bf16 %7, a[20:23], a[12:15], %7\n"
    "ds_read_b128 a[60:63], %17 offset:19456\n"
    "v_mfma_f32_16x16x32_bf16 %8, a[24:27], a[0:3], %8\n"
    "v_mfma_f32_16x16x32_bf16 %9, a[24:27], a[4:7], %9\n"
    "v_mfma_f32_16x16x32_bf16 %10, a[24:27], a[8:11], %10\n"
    "v_mfma_f32_16x16x32_bf16 %11, a[24:27], a[12:15], %11\n"
    "v_mfma_f32_16x16x32_bf16 %12, a[28:31], a[0:3], %12\n"
    "v_mfma_f32_16x16x32_bf16 %13, a[28:31], a[4:7], %13\n"
    "v_mfma_f32_16x16x32_bf16 %14, a[28:31], a[8:11], %14\n"
    "v_mfma_f32_16x16x32_bf16 %15, a[28:31], a[12:15], %15\n"
    "s_waitcnt lgkmcnt(0)\n"
    "s_barrier\n"
    "v_mfma_f32_16x16x32_bf16 %0, a[48:51], a[32:35], %0\n"
    "ds_read_b128 a[0:3], %16 offset:32768\n"
    "v_mfma_f32_16x16x32_bf16 %1, a[48:51], a[36:39], %1\n"
    "ds_read_b128 a[4:7], %16 offset:33792\n"
    "v_mfma_f32_16x16x32_bf16 %2, a[48:51], a[40:43], %2\n"
    "ds_read_b128 a[8:11], %16 offset:34816\n"
    "v_mfma_f32_16x16x32_bf16 %3, a[48:51], a[44:47], %3\n"
    "ds_read_b128 a[12:15], %16 offset:35840\n"
    "v_mfma_f32_16x16x32_bf16 %4, a[52:55], a[32:35], %4\n"
    "ds_read_b128 a[16:19], %17 offset:32768\n"
    "v_mfma_f32_16x16x32_bf16 %5, a[52:55], a[36:39], %5\n"
    "ds_read_b128 a[20:23], %17 offset:33792\n"
    "v_mfma_f32_16x16x32_bf16 %6, a[52:55], a[40:43], %6\n"
    "ds_read_b128 a[24:27], %17 offset:34816\n"
    "v_mfma_f32_16x16x32_bf16 %7, a[52:55], a[44:47], %7\n"
    "ds_read_b128 a[28:31], %17 offset:35840\n"
    "v_mfma_f32_16x16x32_bf16 %8, a[56:59], a[32:35], %8\n"
    "v_mfma_f32_16x16x32_bf16 %9, a[56:59], a[36:39], %9\n"
    "v_mfma_f32_16x16x32_bf16 %10, a[56:59], a[40:43], %10\n"
    "v_mfma_f32_16x16x32_bf16 %11, a[56:59], a[44:47], %11\n"
    "v_mfma_f32_16x16x32_bf16 %12, a[60:63], a[32:35], %12\n"
    "v_mfma_f32_16x16x32_bf16 %13, a[60:63], a[36:39], %13\n"
    "v_mfma_f32_16x16x32_bf16 %14, a[60:63], a[40:43], %14\n"
    "v_mfma_f32_16x16x32_bf16 %15, a[60:63], a[44:47], %15\n"
    "s_waitcnt lgkmcnt(0)\n"
    "s_barrier\n"
    "v_mfma_f32_16x16x32_bf16 %0, a[16:19], a[0:3], %0\n"
    "ds_read_b128 a[32:35], %16 offset:49152\n"
    "v_mfma_f32_16x16x32_bf16 %1, a[16:19], a[4:7], %1\n"
    "ds_read_b128 a[36:39], %16 offset:50176\n"
    "v_mfma_f32_16x16x32_bf16 %2, a[16:19], a[8:11], %2\n"
    "ds_read_b128 a[40:43], %16 offset:51200\n"
    "v_mfma_f32_16x16x32_bf16 %3, a[16:19], a[12:15], %3\n"
    "ds_read_b128 a[44:47], %16 offset:52224\n"
    "v_mfma_f32_16x16x32_bf16 %4, a[20:23], a[0:3], %4\n"
    "ds_read_b128 a[48:51], %17 offset:49152\n"
    "v_mfma_f32_16x16x32_bf16 %5, a[20:23], a[4:7], %5\n"
    "ds_read_b128 a[52:55], %17 offset:50176\n"
    "v_mfma_f32_16x16x32_bf16 %6, a[20:23], a[8:11], %6\n"
    "ds_read_b128 a[56:59], %17 offset:51200\n"
    "v_mfma_f32_16x16x32_bf16 %7, a[20:23], a[12:15], %7\n"
    "ds_read_b128 a[60:63], %17 offset:52224\n"
    "v_mfma_f32_16x16x32_bf16 %8, a[24:27], a[0:3], %8\n"
    "v_mfma_f32_16x16x32_bf16 %9, a[24:27], a[4:7], %9\n"
    "v_mfma_f32_16x16x32_bf16 %10, a[24:27], a[8:11], %10\n"
    "v_mfma_f32_16x16x32_bf16 %11, a[24:27], a[12:15], %11\n"
    "v_mfma_f32_16x16x32_bf16 %12, a[28:31], a[0:3], %12\n"
    "v_mfma_f32_16x16x32_bf16 %13, a[28:31], a[4:7], %13\n"
    "v_mfma_f32_16x16x32_bf16 %14, a[28:31], a[8:11], %14\n"
    "v_mfma_f32_16x16x32_bf16 %15, a[28:31], a[12:15], %15\n"
    "s_waitcnt lgkmcnt(0)\n"
    "s_barrier\n"
    "v_mfma_f32_16x16x32_bf16 %0, a[48:51], a[32:35], %0\n"
    "ds_read_b128 a[0:3], %18 offset:0\n"
    "v_mfma_f32_16x16x32_bf16 %1, a[48:51], a[36:39], %1\n"
    "ds_read_b128 a[4:7], %18 offset:1024\n"
    "v_mfma_f32_16x16x32_bf16 %2, a[48:51], a[40:43], %2\n"
    "ds_read_b128 a[8:11], %18 offset:2048\n"
    "v_mfma_f32_16x16x32_bf16 %3, a[48:51], a[44:47], %3\n"
    "ds_read_b128 a[12:15], %18 offset:3072\n"
    "v_mfma_f32_16x16x32_bf16 %4, a[52:55], a[32:35], %4\n"
    "ds_read_b128 a[16:19], %19 offset:0\n"
    "v_mfma_f32_16x16x32_bf16 %5, a[52:55], a[36:39], %5\n"
    "ds_read_b128 a[20:23], %19 offset:1024\n"
    "v_mfma_f32_16x16x32_bf16 %6, a[52:55], a[40:43], %6\n"
    "ds_read_b128 a[24:27], %19 offset:2048\n"
    "v_mfma_f32_16x16x32_bf16 %7, a[52:55], a[44:47], %7\n"
    "ds_read_b128 a[28:31], %19 offset:3072\n"
    "v_mfma_f32_16x16x32_bf16 %8, a[56:59], a[32:35], %8\n"
    "v_mfma_f32_16x16x32_bf16 %9, a[56:59], a[36:39], %9\n"
    "v_mfma_f32_16x16x32_bf16 %10, a[56:59], a[40:43], %10\n"
    "v_mfma_f32_16x16x32_bf16 %11, a[56:59], a[44:47], %11\n"
    "v_mfma_f32_16x16x32_bf16 %12, a[60:63], a[32:35], %12\n"
    "v_mfma_f32_16x16x32_bf16 %13, a[60:63], a[36:39], %13\n"
    "v_mfma_f32_16x16x32_bf16 %14, a[60:63], a[40:43], %14\n"
    "v_mfma_f32_16x16x32_bf16 %15, a[60:63], a[44:47], %15\n"
    "s_waitcnt lgkmcnt(0)\n"
    "s_barrier\n"
    "v_mfma_f32_16x16x32_bf16 %0, a[16:19], a[0:3], %0\n"
    "ds_read_b128 a[32:35], %18 offset:16384\n"
    "v_mfma_f32_16x16x32_bf16 %1, a[16:19], a[4:7], %1\n"
    "ds_read_b128 a[36:39], %18 offset:17408\n"
    "v_mfma_f32_16x16x32_bf16 %2, a[16:19], a[8:11], %2\n"
    "ds_read_b128 a[40:43], %18 offset:18432\n"
    "v_mfma_f32_16x16x32_bf16 %3, a[16:19], a[12:15], %3\n"
    "ds_read_b128 a[44:47], %18 offset:19456\n"
    "v_mfma_f32_16x16x32_bf16 %4, a[20:23], a[0:3], %4\n"
    "ds_read_b128 a[48:51], %19 offset:16384\n"
    "v_mfma_f32_16x16x32_bf16 %5, a[20:23], a[4:7], %5\n"
    "ds_read_b128 a[52:55], %19 offset:17408\n"
    "v_mfma_f32_16x16x32_bf16 %6, a[20:23], a[8:11], %6\n"
    "ds_read_b128 a[56:59], %19 offset:18432\n"
    "v_mfma_f32_16x16x32_bf16 %7, a[20:23], a[12:15], %7\n"
    "ds_read_b128 a[60:63], %19 offset:19456\n"
    "v_mfma_f32_16x16x32_bf16 %8, a[24:27], a[0:3], %8\n"
    "v_mfma_f32_16x16x32_bf16 %9, a[24:27], a[4:7], %9\n"
    "v_mfma_f32_16x16x32_bf16 %10, a[24:27], a[8:11], %10\n"
    "v_mfma_f32_16x16x32_bf16 %11, a[24:27], a[12:15], %11\n"
    "v_mfma_f32_16x16x32_bf16 %12, a[28:31], a[0:3], %12\n"
    "v_mfma_f32_16x16x32_bf16 %13, a[28:31], a[4:7], %13\n"
    "v_mfma_f32_16x16x32_bf16 %14, a[28:31], a[8:11], %14\n"
    "v_mfma_f32_16x16x32_bf16 %15, a[28:31], a[12:15], %15\n"
    "s_waitcnt lgkmcnt(0)\n"
    "s_barrier\n"
    "v_mfma_f32_16x16x32_bf16 %0, a[48:51], a[32:35], %0\n"
    "ds_read_b128 a[0:3], %18 offset:32768\n"
    "v_mfma_f32_16x16x32_bf16 %1, a[48:51], a[36:39], %1\n"
    "ds_read_b128 a[4:7], %18 offset:33792\n"
    "v_mfma_f32_16x16x32_bf16 %2, a[48:51], a[40:43], %2\n"
    "ds_read_b128 a[8:11], %18 offset:34816\n"
    "v_mfma_f32_16x16x32_bf16 %3, a[48:51], a[44:47], %3\n"
    "ds_read_b128 a[12:15], %18 offset:35840\n"
    "v_mfma_f32_16x16x32_bf16 %4, a[52:55], a[32:35], %4\n"
    "ds_read_b128 a[16:19], %19 offset:32768\n"
    "v_mfma_f32_16x16x32_bf16 %5, a[52:55], a[36:39], %5\n"
    "ds_read_b128 a[20:23], %19 offset:33792\n"
    "v_mfma_f32_16x16x32_bf16 %6, a[52:55], a[40:43], %6\n"
    "ds_read_b128 a[24:27], %19 offset:34816\n"
    "v_mfma_f32_16x16x32_bf16 %7, a[52:55], a[44:47], %7\n"
    "ds_read_b128 a[28:31], %19 offset:35840\n"
    "v_mfma_f32_16x16x32_bf16 %8, a[56:59], a[32:35], %8\n"
    "v_mfma_f32_16x16x32_bf16 %9, a[56:59], a[36:39], %9\n"
    "v_mfma_f32_16x16x32_bf16 %10, a[56:59], a[40:43], %10\n"
    "v_mfma_f32_16x16x32_bf16 %11, a[56:59], a[44:47], %11\n"
    "v_mfma_f32_16x16x32_bf16 %12, a[60:63], a[32:35], %12\n"
    "v_mfma_f32_16x16x32_bf16 %13, a[60:63], a[36:39], %13\n"
    "v_mfma_f32_16x16x32_bf16 %14, a[60:63], a[40:43], %14\n"
    "v_mfma_f32_16x16x32_bf16 %15, a[60:63], a[44:47], %15\n"
    "s_waitcnt lgkmcnt(0)\n"
    "s_barrier\n"
    "v_mfma_f32_16x16x32_bf16 %0, a[16:19], a[0:3], %0\n"
    "ds_read_b128 a[32:35], %18 offset:49152\n"
    "v_mfma_f32_16x16x32_bf16 %1, a[16:19], a[4:7], %1\n"
    "ds_read_b128 a[36:39], %18 offset:50176\n"
    "v_mfma_f32_16x16x32_bf16 %2, a[16:19], a[8:11], %2\n"
    "ds_read_b128 a[40:43], %18 offset:51200\n"
    "v_mfma_f32_16x16x32_bf16 %3, a[16:19], a[12:15], %3\n"
    "ds_read_b128 a[44:47], %18 offset:52224\n"
    "v_mfma_f32_16x16x32_bf16 %4, a[20:23], a[0:3], %4\n"
    "ds_read_b128 a[48:51], %19 offset:49152\n"
    "v_mfma_f32_16x16x32_bf16 %5, a[20:23], a[4:7], %5\n"
    "ds_read_b128 a[52:55], %19 offset:50176\n"
    "v_mfma_f32_16x16x32_bf16 %6, a[20:23], a[8:11], %6\n"
    "ds_read_b128 a[56:59], %19 offset:51200\n"
    "v_mfma_f32_16x16x32_bf16 %7, a[20:23], a[12:15], %7\n"
    "ds_read_b128 a[60:63], %19 offset:52224\n"
    "v_mfma_f32_16x16x32_bf16 %8, a[24:27], a[0:3], %8\n"
    "v_mfma_f32_16x16x32_bf16 %9, a[24:27], a[4:7], %9\n"
    "v_mfma_f32_16x16x32_bf16 %10, a[24:27], a[8:11], %10\n"
    "v_mfma_f32_16x16x32_bf16 %11, a[24:27], a[12:15], %11\n"
    "v_mfma_f32_16x16x32_bf16 %12, a[28:31], a[0:3], %12\n"
    "v_mfma_f32_16x16x32_bf16 %13, a[28:31], a[4:7], %13\n"
    "v_mfma_f32_16x16x32_bf16 %14, a[28:31], a[8:11], %14\n"
    "v_mfma_f32_16x16x32_bf16 %15, a[28:31], a[12:15], %15\n"
    "s_waitcnt lgkmcnt(0)\n"
    "s_barrier\n"
    "v_mfma_f32_16x16x32_bf16 %0, a[48:51], a[32:35], %0\n"
    "ds_read_b128 a[0:3], %16 offset:0\n"
    "v_mfma_f32_16x16x32_bf16 %1, a[48:51], a[36:39], %1\n"
    "ds_read_b128 a[4:7], %16 offset:1024\n"
    "v_mfma_f32_16x16x32_bf16 %2, a[48:51], a[40:43], %2\n"
    "ds_read_b128 a[8:11], %16 offset:2048\n"
    "v_mfma_f32_16x16x32_bf16 %3, a[48:51], a[44:47], %3\n"
    "ds_read_b128 a[12:15], %16 offset:3072\n"
    "v_mfma_f32_16x16x32_bf16 %4, a[52:55], a[32:35], %4\n"
    "ds_read_b128 a[16:19], %17 offset:0\n"
    "v_mfma_f32_16x16x32_bf16 %5, a[52:55], a[36:39], %5\n"
    "ds_read_b128 a[20:23], %17 offset:1024\n"
    "v_mfma_f32_16x16x32_bf16 %6, a[52:55], a[40:43], %6\n"
    "ds_read_b128 a[24:27], %17 offset:2048\n"
    "v_mfma_f32_16x16x32_bf16 %7, a[52:55], a[44:47], %7\n"
    "ds_read_b128 a[28:31], %17 offset:3072\n"
    "v_mfma_f32_16x16x32_bf16 %8, a[56:59], a[32:35], %8\n"
    "v_mfma_f32_16x16x32_bf16 %9, a[56:59], a[36:39], %9\n"
    "v_mfma_f32_16x16x32_bf16 %10, a[56:59], a[40:43], %10\n"
    "v_mfma_f32_16x16x32_bf16 %11, a[56:59], a[44:47], %11\n"
    "v_mfma_f32_16x16x32_bf16 %12, a[60:63], a[32:35], %12\n"
    "v_mfma_f32_16x16x32_bf16 %13, a[60:63], a[36:39], %13\n"
    "v_mfma_f32_16x16x32_bf16 %14, a[60:63], a[40:43], %14\n"
    "v_mfma_f32_16x16x32_bf16 %15, a[60:63], a[44:47], %15\n"
    "s_sub_u32 s40, s40, 1\n"
    "s_cmp_lg_u32 s40, 0\n"
    "s_cbranch_scc1 LG%=\n"
    "s_waitcnt lgkmcnt(0)\n"
    "s_nop 7\n"
      : "+v"(acc[0]), "+v"(acc[1]), "+v"(acc[2]), "+v"(acc[3]), "+v"(acc[4]), "+v"(acc[5]), "+v"(acc[6]), "+v"(acc[7]),
        "+v"(acc[8]), "+v"(acc[9]), "+v"(acc[10]), "+v"(acc[11]), "+v"(acc[12]), "+v"(acc[13]), "+v"(acc[14]), "+v"(acc[15])
      : "v"(ra), "v"(rw), "v"(ra + 65536), "v"(rw + 65536)
      : "memory", "scc", "s40", "a0", "a1", "a2", "a3", "a4", "a5", "a6", "a7", "a8", "a9", "a10", "a11", "a12", "a13", "a14", "a15", "a16", "a17", "a18", "a19", "a20", "a21", "a22", "a23", "a24", "a25", "a26", "a27", "a28", "a29", "a30", "a31", "a32", "a33", "a34", "a35", "a36", "a37", "a38", "a39", "a40", "a41", "a42", "a43", "a44", "a45", "a46", "a47", "a48", "a49", "a50", "a51", "a52", "a53", "a54", "a55", "a56", "a57", "a58", "a59", "a60", "a61", "a62", "a63");
  }
  const __amdgpu_buffer_rsrc_t rp = __builtin_amdgcn_make_buffer_rsrc(ws + WS_P + (size_t)tile * (131072 + 5120), 0, 131072, 0x20000);
  unsigned* flags = (unsigned*)(ws + WS_F) + 16 * tile;
  if (wave != ks) {
#pragma unroll
    for (int n = 0; n < 8; ++n) {
      const f32x4 a0 = acc[8 * (n & 1) + (n >> 1)], a1 = acc[8 * (n & 1) + 4 + (n >> 1)];
      __builtin_amdgcn_raw_buffer_store_b128(u32x4{pk(a0[0], a0[1]), pk(a0[2], a0[3]), pk(a1[0], a1[1]), pk(a1[2], a1[3])}, rp, 16 * lane + 32768 * wave + 8192 * ks + 1024 * n, 0, 16);
    }
    asm volatile("s_waitcnt vmcnt(0)" ::: "memory");
    if (lane == 0) __hip_atomic_store(flags + 4 * wave + ks, 1u, __ATOMIC_RELAXED, __HIP_MEMORY_SCOPE_AGENT);
  } else {
#pragma unroll
    for (int n = 0; n < 16; ++n) *(f32x4*)(lds + 1024 * n + 16 * lane) = acc[n];
  }
  const int G = lane >> 4, row = 128 * rt + 64 * (ks >> 1) + 16 * wave + (lane & 15), col = 128 * ct + 64 * (ks & 1) + 16 * (G & 1) + 8 * (G >> 1);
  const u32x4 xq[2] = {*(const u32x4*)(x + row * 512 + col), *(const u32x4*)(x + row * 512 + col + 32)};
  const f32x2 st = stats[row];
  const float rd = __builtin_amdgcn_rcpf(st[1]);
  unsigned gp[8];
#pragma unroll
  for (int j = 0; j < 8; ++j) {
    const unsigned w = xq[j >> 2][j & 3];
    const float z0 = lnv(blo(w), st[0], rd), z1 = lnv(bhi(w), st[0], rd);
    gp[j] = pk(z0 * sigm(z0), z1 * sigm(z1));
    asm volatile("" : "+v"(gp[j]));
  }
  __syncthreads();
  if (row == *idxsh) {
#pragma unroll
    for (int j = 0; j < 8; ++j) {
      const unsigned w = xq[j >> 2][j & 3];
      const int c = (col & 63) + 32 * (j >> 2) + 2 * (j & 3);
      const float z0 = bfr(lnv(blo(w), st[0], rd) + ysh[c]), z1 = bfr(lnv(bhi(w), st[0], rd) + ysh[c + 1]);
      gp[j] = pk(z0 * sigm(z0), z1 * sigm(z1));
    }
  }
  f32x4 s[4];
#pragma unroll
  for (int j = 0; j < 4; ++j) s[j] = *(const f32x4*)(lds + 1024 * (4 * j + wave) + 16 * lane);
  for (int it = 0; it < (1 << 20); ++it) {
    const unsigned f = __hip_atomic_load(flags + 4 * ks + (lane & 3), __ATOMIC_RELAXED, __HIP_MEMORY_SCOPE_AGENT);
    if (__all(f == 1u || (lane & 3) == ks)) break;
    __builtin_amdgcn_s_sleep(1);
  }
  asm volatile("" ::: "memory");
  u32x4 p[3][2];
#pragma unroll
  for (int t = 0; t < 3; ++t)
#pragma unroll
    for (int hf = 0; hf < 2; ++hf) p[t][hf] = __builtin_amdgcn_raw_buffer_load_b128(rp, 16 * lane, 32768 * ks + 8192 * ((ks + 1 + t) & 3) + 1024 * (2 * wave + hf), 16);
  const __amdgpu_buffer_rsrc_t rout = __builtin_amdgcn_make_buffer_rsrc(out, 0, 2097152, 0x20000);
#pragma unroll
  for (int hf = 0; hf < 2; ++hf) {
    f32x4 s0 = s[2 * hf], s1 = s[2 * hf + 1];
#pragma unroll
    for (int t = 0; t < 3; ++t) {
      const u32x4 v = p[t][hf];
      s0 += f32x4{blo(v[0]), bhi(v[0]), blo(v[1]), bhi(v[1])}; s1 += f32x4{blo(v[2]), bhi(v[2]), blo(v[3]), bhi(v[3])};
    }
    const auto e0 = __builtin_amdgcn_permlane16_swap(pk(s0[0], s0[1]), pk(s1[0], s1[1]), false, false);
    const auto e1 = __builtin_amdgcn_permlane16_swap(pk(s0[2], s0[3]), pk(s1[2], s1[3]), false, false);
    const unsigned sp[4] = {e0[0], e1[0], e0[1], e1[1]};
    u32x4 o;
#pragma unroll
    for (int j = 0; j < 4; ++j) {
      const unsigned w = xq[hf][j], gg = gp[4 * hf + j], t = pk(blo(sp[j]) * blo(gg), bhi(sp[j]) * bhi(gg));
      o[j] = pk(blo(w) + blo(t), bhi(w) + bhi(t));
    }
    __builtin_amdgcn_raw_buffer_store_b128(o, rout, (row * 512 + col + 32 * hf) * 2, 0, 0);
  }
}

extern "C" void kernel_launch(void* const* d_in, const int* in_sizes, int n_in, void* d_out, int out_size, void* d_ws, size_t ws_size, hipStream_t stream) {
  k_attn<<<256, 256, 0, stream>>>(d_in[0], d_in[1], d_in[2], d_in[3], d_in[4], d_in[6], d_ws, d_in[7], d_in[8]);
  k_out<<<256, 448, 0, stream>>>((const unsigned short*)d_in[0], (const unsigned short*)d_in[9], (char*)d_ws,
                                 (unsigned short*)d_out);
}
```
